```python
import torch

def _softmax(x, axis=-1):
    return torch.softmax(x, dim=axis)
def _gelu(x):
    return torch.nn.functional.gelu(x, approximate="tanh")
def _silu(x):
    return torch.nn.functional.silu(x)
def _sigmoid(x):
    return torch.sigmoid(x)
def _layernorm(x, axis=-1, eps=1e-5):
    m = x.mean(dim=axis, keepdim=True)
    v = x.var(dim=axis, keepdim=True, unbiased=False)
    return (x - m) / torch.sqrt(v + eps)
def _scatter_add(tgt, idx, src, axis=0):
    return tgt.clone().index_add_(axis, idx, src)
def _segment_sum(src, seg, n):
    out = torch.zeros((n,) + src.shape[1:], dtype=src.dtype)
    return out.index_add_(0, seg, src)
def _pad(x, pw):
    flat = tuple(v for pair in reversed(pw) for v in pair)
    return torch.nn.functional.pad(x, flat)

kernel_name = 'recurrent_scan__sliding_block__moe_arch_mod__prompt'

def setup_inputs():

    _w = (lambda a: (lambda t: t.to(torch.bfloat16) if t.is_floating_point() else t)(torch.as_tensor(a)))
    def _A_setup():
        B, S, D = 1, 2048, 512
        return {
            "x": _w((lambda *s: torch.randn(*s, dtype=torch.bfloat16) * 0.05)(B, S, D)),
            "wqkv": _w((lambda *s: torch.randn(*s, dtype=torch.bfloat16) * 0.05)(D, 3 * D)),
            "wo": _w((lambda *s: torch.randn(*s, dtype=torch.bfloat16) * 0.05)(D, D)),
        }
    _A_in = _A_setup()

    def _B_setup():
        B, S, D, F, C = 1, 2048, 512, 1792, 8
        return {
            "x": _w((lambda *s: torch.randn(*s, dtype=torch.bfloat16) * 0.05)(B, S, D)),
            "w_r": _w((lambda *s: torch.randn(*s, dtype=torch.bfloat16) * 0.05)(D)),
            "w_gate": _w((lambda *s: torch.randn(*s, dtype=torch.bfloat16) * 0.05)(D, F)),
            "w_up": _w((lambda *s: torch.randn(*s, dtype=torch.bfloat16) * 0.05)(D, F)),
            "w_down": _w((lambda *s: torch.randn(*s, dtype=torch.bfloat16) * 0.05)(F, D)),
        }
    _B_in = _B_setup()

    return {
        "A_x": _A_in["x"],
        "A_wqkv": _A_in["wqkv"],
        "A_wo": _A_in["wo"],
        "B_x": _B_in["x"],
        "B_w_r": _B_in["w_r"],
        "B_w_gate": _B_in["w_gate"],
        "B_w_up": _B_in["w_up"],
        "B_w_down": _B_in["w_down"],
    }

def reference(A_x, A_wqkv, A_wo, B_x, B_w_r, B_w_gate, B_w_up, B_w_down):
    def _A_ref(x, wqkv, wo):
        B, S, D = x.shape
        H = 32; Dh = D // H; W = 256
        qkv = (lambda x, *d: x.permute(*d))((x @ wqkv).reshape(B, S, 3, H, Dh), 2, 0, 3, 1, 4)
        q, k, v = qkv[0], qkv[1], qkv[2]
        scores = torch.einsum("bhqd,bhkd->bhqk", q, k) * (Dh ** -0.5)
        idx = torch.arange(S)
        mask = (idx[:, None] - idx[None, :] >= 0) & (idx[:, None] - idx[None, :] < W)
        scores = torch.where(mask[None, None], scores, -1e9)
        p = _softmax(scores, axis=-1)
        o = torch.einsum("bhqk,bhkd->bhqd", p, v)
        return (lambda x, *d: x.permute(*d))(o, 0, 2, 1, 3).reshape(B, S, D) @ wo

    def _B_ref(x, w_r, w_gate, w_up, w_down):
        B, S, D = x.shape
        C = 8
        r = x @ w_r
        idx = torch.argsort(-r, axis=-1)[:, :C]
        idx_e = (lambda x, n, axis: torch.repeat_interleave(x, n, dim=axis))(idx.reshape(B, C, 1), D, axis=2)
        x_sel = (lambda x, i, axis: torch.take_along_dim(x, i, dim=axis))(x, idx_e, axis=1)
        g = _silu(x_sel @ w_gate)
        u = x_sel @ w_up
        y = (g * u) @ w_down
        r_sel = _sigmoid((lambda x, i, axis: torch.take_along_dim(x, i, dim=axis))(r, idx, axis=1))
        y = y * r_sel.reshape(B, C, 1)
        b_off = (torch.arange(B) * S).reshape(B, 1)
        flat_idx = (idx + b_off).reshape(B * C)
        zero = (x * 0.0).reshape(B * S, D)
        upd = _scatter_add(zero, flat_idx, y.reshape(B * C, D), axis=0)
        return x + upd.reshape(B, S, D)

    x = A_x
    xn = _layernorm(x, axis=-1)
    s = _A_ref(xn, A_wqkv, A_wo)
    g = _silu(_B_ref(xn, B_w_r, B_w_gate, B_w_up, B_w_down))
    return x + s * g
```

```cpp
#include <hip/hip_runtime.h>
#include <hip/hip_bf16.h>
#include <stdint.h>

typedef unsigned short u16;
__device__ __forceinline__ float bf2f(u16 v) { return __uint_as_float(((unsigned)v) << 16); }
__device__ __forceinline__ u16 f2bf(float f) { unsigned u = __float_as_uint(f); u += 0x7fffu + ((u >> 16) & 1u); return (u16)(u >> 16); }
__device__ __forceinline__ float rbf(float f) { return bf2f(f2bf(f)); }

constexpr int S = 2048, D = 512, F = 1792, H = 32, DH = 16, W = 256;

__global__ void k_ln(const u16* x, const u16* wr, u16* xn, float* r) {
    int row = blockIdx.x; int l = threadIdx.x;
    float v[8]; float s = 0;
    for (int j = 0; j < 8; j++) { v[j] = bf2f(x[row * D + l * 8 + j]); s += v[j]; }
    for (int o = 32; o; o >>= 1) s += __shfl_xor(s, o);
    float mean = s / D;
    float q = 0;
    for (int j = 0; j < 8; j++) { float d = v[j] - mean; q += d * d; }
    for (int o = 32; o; o >>= 1) q += __shfl_xor(q, o);
    float var = rbf(q / D);
    float m = rbf(mean);
    float sd = rbf(sqrtf(rbf(var + 1e-5f)));
    float acc = 0;
    for (int j = 0; j < 8; j++) { float t = rbf(rbf(v[j] - m) / sd); xn[row * D + l * 8 + j] = f2bf(t); acc += t * bf2f(wr[l * 8 + j]); }
    for (int o = 32; o; o >>= 1) acc += __shfl_xor(acc, o);
    if (l == 0) r[row] = rbf(acc);
}
__global__ void k_gemm(const u16* A, const u16* B, u16* C, int M, int N, int K) {
    int n = blockIdx.x * 64 + threadIdx.x; int m = blockIdx.y;
    float acc = 0;
    for (int k = 0; k < K; k++) acc += bf2f(A[m * K + k]) * bf2f(B[k * N + n]);
    C[m * N + n] = f2bf(acc);
}
__global__ void k_attn(const u16* qkv, u16* o) {
    int row = blockIdx.x, h = blockIdx.y; int t = threadIdx.x;
    __shared__ float p[256]; __shared__ float red[4];
    int key = row - t;
    float sc = -1e30f;
    if (key >= 0) { float a = 0; for (int d = 0; d < DH; d++) a += bf2f(qkv[row * 1536 + h * 16 + d]) * bf2f(qkv[key * 1536 + 512 + h * 16 + d]); sc = rbf(a) * 0.25f; }
    float mx = sc; for (int o2 = 32; o2; o2 >>= 1) mx = fmaxf(mx, __shfl_xor(mx, o2));
    if ((t & 63) == 0) red[t >> 6] = mx; __syncthreads();
    mx = fmaxf(fmaxf(red[0], red[1]), fmaxf(red[2], red[3])); __syncthreads();
    float e = key >= 0 ? __expf(sc - mx) : 0.f;
    float sm = e; for (int o2 = 32; o2; o2 >>= 1) sm += __shfl_xor(sm, o2);
    if ((t & 63) == 0) red[t >> 6] = sm; __syncthreads();
    sm = red[0] + red[1] + red[2] + red[3];
    p[t] = rbf(e / sm); __syncthreads();
    if (t < 16) { float a = 0; for (int j = 0; j < 256; j++) { int k2 = row - j; if (k2 >= 0) a += p[j] * bf2f(qkv[k2 * 1536 + 1024 + h * 16 + t]); } o[row * D + h * 16 + t] = f2bf(a); }
}
__global__ void k_top(const float* r, int* idx) {
    __shared__ float v[S]; int t = threadIdx.x;
    for (int i = t; i < S; i += 256) v[i] = r[i];
    __syncthreads();
    for (int i = t; i < S; i += 256) { int rank = 0; float x = v[i]; for (int j = 0; j < S; j++) rank += (v[j] > x) || (v[j] == x && j < i); if (rank < 8) idx[rank] = i; }
}
__global__ void k_gu(const u16* xn, const int* idx, const u16* wg, const u16* wu, u16* hbuf) {
    int n = blockIdx.x * 64 + threadIdx.x; int c = blockIdx.y; int row = idx[c];
    float a = 0, b = 0;
    for (int k = 0; k < D; k++) { float xv = bf2f(xn[row * D + k]); a += xv * bf2f(wg[k * F + n]); b += xv * bf2f(wu[k * F + n]); }
    a = rbf(a); b = rbf(b);
    float g = rbf(a / (1.f + __expf(-a)));
    hbuf[c * F + n] = f2bf(g * b);
}
__global__ void k_down(const u16* hbuf, const u16* wd, const int* idx, const float* r, u16* xb) {
    int n = blockIdx.x * 64 + threadIdx.x; int c = blockIdx.y; int row = idx[c];
    float a = 0;
    for (int k = 0; k < F; k++) a += bf2f(hbuf[c * F + k]) * bf2f(wd[k * D + n]);
    a = rbf(a);
    float rs = rbf(1.f / (1.f + __expf(-r[row])));
    float y = rbf(a * rs);
    xb[row * D + n] = f2bf(bf2f(xb[row * D + n]) + y);
}
__global__ void k_fin(const u16* x, const u16* s, const u16* xb, u16* out) {
    int i = blockIdx.x * 256 + threadIdx.x;
    float b = bf2f(xb[i]); float g = rbf(b / (1.f + __expf(-b)));
    out[i] = f2bf(bf2f(x[i]) + rbf(bf2f(s[i]) * g));
}

extern "C" void kernel_launch(void* const* d_in, const int* in_sizes, int n_in, void* d_out, int out_size, void* d_ws, size_t ws_size, hipStream_t stream) {
    const u16* x = (const u16*)d_in[0]; const u16* wqkv = (const u16*)d_in[1]; const u16* wo = (const u16*)d_in[2];
    const u16* wr = (const u16*)d_in[4]; const u16* wg = (const u16*)d_in[5]; const u16* wu = (const u16*)d_in[6]; const u16* wd = (const u16*)d_in[7];
    char* ws = (char*)d_ws;
    u16* xn = (u16*)ws; u16* qkv = (u16*)(ws + (4 << 20)); u16* o = (u16*)(ws + (12 << 20)); u16* s = (u16*)(ws + (16 << 20));
    float* r = (float*)(ws + (20 << 20)); int* idx = (int*)(ws + (21 << 20)); u16* hb = (u16*)(ws + (22 << 20));
    k_ln<<<S, 64, 0, stream>>>(x, wr, xn, r);
    k_gemm<<<dim3(1536 / 64, S), 64, 0, stream>>>(xn, wqkv, qkv, S, 1536, D);
    k_attn<<<dim3(S, H), 256, 0, stream>>>(qkv, o);
    k_gemm<<<dim3(D / 64, S), 64, 0, stream>>>(o, wo, s, S, D, D);
    k_top<<<1, 256, 0, stream>>>(r, idx);
    k_gu<<<dim3(F / 64, 8), 64, 0, stream>>>(xn, idx, wg, wu, hb);
    k_down<<<dim3(D / 64, 8), 64, 0, stream>>>(hb, wd, idx, r, xn);
    k_fin<<<S * D / 256, 256, 0, stream>>>(x, s, xn, (u16*)d_out);
}
```

```cpp
#include <hip/hip_runtime.h>
#include <stdint.h>

typedef unsigned short u16;
typedef unsigned u32;
typedef unsigned long long u64;
typedef u32 u32x4 __attribute__((ext_vector_type(4)));
typedef u32 u32x2 __attribute__((ext_vector_type(2)));
typedef int i32x4 __attribute__((ext_vector_type(4)));
typedef __bf16 bf16x8 __attribute__((ext_vector_type(8)));
typedef __bf16 bf16x4 __attribute__((ext_vector_type(4)));
typedef __bf16 bf16x2 __attribute__((ext_vector_type(2)));
typedef float f32x16 __attribute__((ext_vector_type(16)));
typedef float f32x4 __attribute__((ext_vector_type(4)));
typedef float f32x2 __attribute__((ext_vector_type(2)));
#define LAS __attribute__((address_space(3)))
#define GAS __attribute__((address_space(1)))
#define DI __device__ __forceinline__
#define RLX_AGENT __ATOMIC_RELAXED, __HIP_MEMORY_SCOPE_AGENT

constexpr u32 WS_EPOCH = 0;
constexpr u32 FS = 64;
constexpr u32 WS_FLAGK = 128 << 10;
constexpr u32 WS_FLAGV = 256 << 10;
constexpr u32 WS_FLAGO = 384 << 10;
constexpr u32 WS_ONES = 512 << 10;
constexpr u32 WS_R = 64 << 10;
constexpr u32 WS_S = 80 << 10;
constexpr u32 WS_FGU = 96 << 10;
constexpr u32 WS_GU = 7 << 20;
constexpr u32 WS_Y = 8 << 20;
constexpr u32 WS_K = 1 << 20;
constexpr u32 WS_V = 3 << 20;
constexpr u32 WS_O = 5 << 20;
constexpr u32 WS_USED = 9 << 20;
#define STAMP(i) do {} while (0)

#define STAMPS_STEP(n) do {} while (0)
constexpr u32 SL = 8192, GR = 8 * SL;
constexpr u32 L_SCR = 2 * GR;
constexpr u32 L_OWN = L_SCR;
constexpr u32 L_Q = L_SCR + 16384;
constexpr u32 L_M = L_SCR + 24576;
constexpr u32 L_STP = L_SCR + 25600;
constexpr u32 L_CS = L_SCR + 26624;
constexpr u32 L_L = L_SCR + 25600;
constexpr u32 L_ST = L_SCR + 28160;
constexpr u32 L_FLG = L_SCR + 28672;
constexpr u32 L_ONES = L_SCR + 29184;
constexpr u32 L_SEL = L_SCR + 30208;
constexpr u32 L_H = L_SCR + 30720;
constexpr u32 L_YR = 98304;
constexpr u32 L_OST = 65536;
constexpr u32 L_TR = 0;
constexpr u32 L_TOTAL = 163840;
__device__ constexpr u32 fill_slot(int f) { return f < 4 ? 4 + f : ((f - 4) >> 1) + ((f & 1) ? 4 : 0); }
constexpr u32 F_CS = 0;
constexpr u32 F_Q = 1;
constexpr u32 F_LAND = 2;
constexpr u32 F_HALO = 3;
constexpr u32 F_O = 8;
constexpr u32 F_SEL = 16;
constexpr u32 F_TAG = 25;

DI float bf_lo(u32 v) { return __uint_as_float(v << 16); }
DI float bf_hi(u32 v) { return __uint_as_float(v & 0xffff0000u); }
DI u32 pk_bf16(float a, float b) { f32x2 f = {a, b}; return __builtin_bit_cast(u32, __builtin_convertvector(f, bf16x2)); }
DI float rbf(float a) { return bf_lo(pk_bf16(a, a)); }
DI float dot2(u32 a, u32 b, float c) { return __builtin_amdgcn_fdot2_f32_bf16(__builtin_bit_cast(bf16x2, a), __builtin_bit_cast(bf16x2, b), c, false); }
DI f32x16 mfma32(u32x4 a, u32x4 b, f32x16 c) { return __builtin_amdgcn_mfma_f32_32x32x16_bf16(__builtin_bit_cast(bf16x8, a), __builtin_bit_cast(bf16x8, b), c, 0, 0, 0); }
DI f32x4 mfma16(u32x4 a, u32x4 b, f32x4 c) { return __builtin_amdgcn_mfma_f32_16x16x32_bf16(__builtin_bit_cast(bf16x8, a), __builtin_bit_cast(bf16x8, b), c, 0, 0, 0); }
DI u32x2 tr_read(u32 addr) { return __builtin_bit_cast(u32x2, __builtin_amdgcn_ds_read_tr16_b64_v4bf16((LAS bf16x4*)addr)); }
DI u32x4 lds_r128(u32 addr) { return *(LAS u32x4*)addr; }
DI f32x4 lds_rf4(u32 addr) { return *(LAS f32x4*)addr; }
DI void lds_w128(u32 addr, u32x4 v) { *(LAS u32x4*)addr = v; }
DI void lds_wf4(u32 addr, f32x4 v) { *(LAS f32x4*)addr = v; }
template <int CTRL> DI float dpp_add(float v) { return v + __uint_as_float(__builtin_amdgcn_update_dpp(0u, __float_as_uint(v), CTRL, 0xf, 0xf, true)); }
DI float red8(float v) { v = dpp_add<0xB1>(v); v = dpp_add<0x4E>(v); return dpp_add<0x141>(v); }
DI i32x4 mk_rsrc(const void* p, u32 bytes) { u64 a = (u64)p; i32x4 r; r.x = (int)(u32)a; r.y = (int)(u32)(a >> 32) & 0xffff; r.z = (int)bytes; r.w = 0x00020000; return r; }
DI void dma16(u32 lds, u32 voff, i32x4 rsrc, u32 soff) {
    asm volatile("s_mov_b32 m0, %0\n\ts_nop 0\n\tbuffer_load_dwordx4 %1, %2, %3 offen lds" :: "s"(lds), "v"(voff), "s"(rsrc), "s"(soff) : "memory");
}
DI u32 opaque(u32 v) { asm volatile("" : "+v"(v)); return v; }
DI u32x4 pack8(f32x4 a, f32x4 b) { return (u32x4){pk_bf16(a.x, a.y), pk_bf16(a.z, a.w), pk_bf16(b.x, b.y), pk_bf16(b.z, b.w)}; }
#define Q4(acc, q) ((f32x4){acc[4 * (q)], acc[4 * (q) + 1], acc[4 * (q) + 2], acc[4 * (q) + 3]})
#define BAR() asm volatile("s_waitcnt lgkmcnt(0)\n\ts_barrier" ::: "memory")
#define RBAR() asm volatile("s_barrier" ::: "memory")
#define WAITV_(n) asm volatile("s_waitcnt vmcnt(" #n ")" ::: "memory")
#define WAITV(n) ({ WAITV_(n); })
constexpr u32 SPIN_CAP = 1u << 20;
#define XDELAY 6
#define WOEARLY 0
#define FORCE_FB 0
#define ROTX 2
#define WOQ 4
constexpr u32 ONES = 0x3f803f80u;

#define SB() __builtin_amdgcn_sched_barrier(0)
DI float np(float r) { asm("" : "+v"(r)); return r; }
DI u32x2 scl4(f32x4 a, f32x4 r) { return (u32x2){pk_bf16(np(a.x * r.x), np(a.y * r.y)), pk_bf16(np(a.z * r.z), np(a.w * r.w))}; }
DI u32x4 split3(float c) {
    const u32 a = pk_bf16(c, c); const float r1 = np(c - bf_lo(a));
    const u32 b = pk_bf16(r1, r1); const float r2 = np(r1 - bf_lo(b));
    return (u32x4){(a & 0xffffu) | (b & 0xffff0000u), pk_bf16(r2, 0.f), 0u, 0u};
}
DI f32x4 addmul4(f32x4 a, f32x4 b, float m) { return (f32x4){np(np(a.x + b.x) * m), np(np(a.y + b.y) * m), np(np(a.z + b.z) * m), np(np(a.w + b.w) * m)}; }
DI f32x4 add4(f32x4 a, f32x4 b) { return (f32x4){np(a.x + b.x), np(a.y + b.y), np(a.z + b.z), np(a.w + b.w)}; }
DI f32x4 bc4(float a) { return (f32x4){a, a, a, a}; }
DI u32x4 cat(u32x2 a, u32x2 b) { return (u32x4){a.x, a.y, b.x, b.y}; }
#define PIN4(v) asm volatile("" : "+v"(v))
DI void lds_sig_(u32& pt, u32 addr) {
    asm volatile("v_mov_b32 %0, %1\n\ts_mov_b64 exec, 1\n\tds_add_u32 %0, %0\n\ts_mov_b64 exec, -1" : "+v"(pt) : "s"(addr) : "memory");
}
DI void lds_need_(u32& pt, u32& have, u32 addr, u32 n) {
    u32 c;
    asm volatile("s_cmp_lt_u32 %0, %4\n\ts_cbranch_scc1 3f\n2:\n\t.subsection 1\n3:\n\ts_mov_b32 %2, 0x400000\n"
                 "1:\n\tv_mov_b32 %1, %3\n\tds_read_b32 %1, %1\n\ts_waitcnt lgkmcnt(0)\n\tv_readfirstlane_b32 %0, %1\n\ts_cmp_ge_u32 %0, %4\n\ts_cbranch_scc1 2b\n\ts_sub_u32 %2, %2, 1\n\ts_cbranch_scc0 1b\n\ts_branch 2b\n\t.subsection 0"
                 : "+s"(have), "+v"(pt), "=&s"(c) : "s"(addr), "s"(n) : "memory", "scc");
}
DI void lds_cnt_(u32& pt, u32 addr, u32 n) {
    u32 c, v;
    asm volatile("s_mov_b32 %2, 0x400000\n"
                 "1:\n\tv_mov_b32 %1, %3\n\tds_read_b32 %1, %1\n\ts_waitcnt lgkmcnt(0)\n\tv_readfirstlane_b32 %0, %1\n\ts_cmp_ge_u32 %0, %4\n\ts_cbranch_scc1 2f\n\ts_sub_u32 %2, %2, 1\n\ts_cbranch_scc0 1b\n2:"
                 : "=&s"(v), "+v"(pt), "=&s"(c) : "s"(addr), "s"(n * addr) : "memory", "scc");
}
DI void lds_bits_(u32& pt, u32 addr, u32 bits) {
    u32 c, v;
    asm volatile("s_mov_b32 %2, 0x400000\n"
                 "1:\n\tv_mov_b32 %1, %3\n\tds_read_b32 %1, %1\n\ts_waitcnt lgkmcnt(0)\n\tv_readfirstlane_b32 %0, %1\n\ts_andn2_b32 %0, %4, %0\n\ts_cbranch_scc0 2f\n\ts_sub_u32 %2, %2, 1\n\ts_cbranch_scc0 1b\n2:"
                 : "=&s"(v), "+v"(pt), "=&s"(c) : "s"(addr), "s"(bits) : "memory", "scc");
}
#define lds_sig(a) lds_sig_(pt, a)
#define lds_need(h, a, n) lds_need_(pt, h, a, n)
#define lds_cnt(a, n) lds_cnt_(pt, a, n)
#define lds_bits(a, b) lds_bits_(pt, a, b)
DI f32x16 mfp(u32x4 a, u32x4 b, f32x16 c) { f32x16 r = mfma32(a, b, c); PIN4(r); return r; }
DI u32 pkp(float a, float b) { return opaque(pk_bf16(a, b)); }
template <int CTRL, int RM> DI u32 dpp_umax(u32 v) { const u32 o = __builtin_amdgcn_update_dpp(0u, v, CTRL, RM, 0xf, false); return v > o ? v : o; }
DI u32 wave_umax(u32 v) { v = dpp_umax<0x111, 0xf>(v); v = dpp_umax<0x112, 0xf>(v); v = dpp_umax<0x114, 0xf>(v); v = dpp_umax<0x118, 0xf>(v); v = dpp_umax<0x142, 0xa>(v); v = dpp_umax<0x143, 0xc>(v);
    return __builtin_amdgcn_readlane(v, 63); }
DI float silu_(float x) { return x * __builtin_amdgcn_rcpf(1.0f + __builtin_amdgcn_exp2f(x * -1.4426950408889634f)); }
DI u32 norm2(u32 x, float m, float rs) { const u32 d = pk_bf16(bf_lo(x) - m, bf_hi(x) - m); return pk_bf16(bf_lo(d) * rs, bf_hi(d) * rs); }
DI void st_sc1(__amdgpu_buffer_rsrc_t r, u32 off, u32x4 v) { __builtin_amdgcn_raw_buffer_store_b128(v, r, off, 0, 16); }
DI float swap_add(float m) { auto sw = __builtin_amdgcn_permlane32_swap(__float_as_uint(m), __float_as_uint(m), false, false); return __uint_as_float(sw[0]) + __uint_as_float(sw[1]); }
DI float lds_rf(u32 a) { return *(LAS float*)a; }
DI void lds_wf(u32 a, float v) { *(LAS float*)a = v; }

__global__ __launch_bounds__(512, 2) void fused_kernel(const u16* __restrict__ x, const u16* __restrict__ wqkv, char* __restrict__ ws, const u16* __restrict__ wo, u16* __restrict__ out,
                                                        const u16* __restrict__ wr, const u16* __restrict__ wg, const u16* __restrict__ wu, const u16* __restrict__ wd) {
    asm volatile("" :: "s"(x), "s"(wqkv), "s"(ws), "s"(wo));
    __shared__ __attribute__((aligned(1024))) char lds_mem[L_TOTAL];
    const u32 LB = (u32)(uintptr_t)lds_mem;
    const int b = blockIdx.x, xcd = b & 7, bi = b >> 3;
    const int rb = (xcd >> 1) * 8 + (bi >> 2), hq = (xcd & 1) * 4 + (bi & 3);
    const int tid = threadIdx.x, lane = tid & 63;
    const int wv = __builtin_amdgcn_readfirstlane(tid >> 6);
    const int rot = (rb + ROTX * hq) & 3;
    const int h = lane >> 5, l31 = lane & 31;
#define STAMP_OUT() do {} while (0)
    STAMP(0);

    const u32 flg = LB + L_FLG;
    const i32x4 rs_ws = mk_rsrc(ws, WS_USED);
    const __amdgpu_buffer_rsrc_t bws = __builtin_amdgcn_make_buffer_rsrc(ws, 0, WS_USED, 0x00020000);
    const u32 l16 = opaque(lane * 16);
    u32 spins = 0, pt = 0;
    u32 tag;
    f32x16 acc, zero;
#pragma unroll
    for (int i = 0; i < 16; i++) zero[i] = 0.f;
    const int cw = wv & 3, rg = cw & 1, pr = cw >> 1, hd = wv < 4 ? 1 : 0;
    const int g = rb * 2 + rg, pair = hq * 2 + pr;
    const int g0 = rb * 2 - 8 + rg * 4;
    const u32 own = LB + L_OWN + cw * 4096;
    const u32 vlo = h * 256 + (lane & 15) * 16;
    u32 hso[4];
    u32 dmask[8];
    u32x4 kf[4];
    u32x4 vf[4][2];
    const u32 kbits = 0xfu << (pr * 8 + rg * 4), vbits = kbits << 16;
    auto prep = [&]() {
#pragma unroll
        for (int t = 0; t < 4; t++) { hso[t] = g0 + t >= 0 ? (u32)((g0 + t) * 32 + pair * 2 + hd) * 1024 : 0x40000000u; asm volatile("" : "+s"(hso[t])); }
        const u32 mbits = (u32)((2ull << l31) - 1ull) >> (4 * h);
#pragma unroll
        for (int i = 0; i < 8; i++) { const int k = ((2 * i) & 3) + 8 * (i >> 1);
            dmask[i] = opaque(((u32)__builtin_amdgcn_sbfe(mbits, k, 1) & 0xffffu) | ((u32)__builtin_amdgcn_sbfe(mbits, k + 1, 1) & 0xffff0000u)); }
    };
    auto ld_k = [&]() {
#pragma unroll
        for (int t = 0; t < 4; t++) kf[t] = __builtin_amdgcn_raw_buffer_load_b128(bws, l16, WS_K + hso[t], 16); };
    auto ld_v = [&]() {
        const u32 vab = (lane & 16) ? WS_ONES + b * 1024 : WS_V + vlo, vam = (lane & 16) ? 0x40000000u : 0xffffffffu;
#pragma unroll
        for (int tt = 0; tt < 4; tt++) { const int t = (tt + 2) & 3; const u32 va = vab + (hso[t] & vam);
            vf[t][0] = __builtin_amdgcn_raw_buffer_load_b128(bws, va, 0, 16); vf[t][1] = __builtin_amdgcn_raw_buffer_load_b128(bws, va + 512, 0, 16); } };
    auto st_flag = [&](u32 off) { __builtin_amdgcn_raw_buffer_store_b32(tag, bws, 0, off, 16); };

    auto fill_o = [&](int sl, int j0, int j1) {
        const i32x4 ro = mk_rsrc(wo, 512 * 512 * 2);
        const u32 vo_w = (u32)(lane >> 4) * 1024 + (((lane & 15) ^ (((lane >> 4) & 3) << 2)) << 4);
        u32 so = (u32)(sl ^ (rb & 7)) * 32768 + (hq >> 1) * 256 + j0 * 4096, ld = LB + sl * SL + j0 * 1024;
#pragma unroll
        for (int j = j0; j < j1; j++) { dma16(ld, vo_w, ro, so); ld += 1024; so += 4096; }
    };
    if (__builtin_expect(wv < 4, 1)) {
        if (wv < 2) {
            __builtin_amdgcn_s_setprio(3);
            const int lw = wv;
            const u32 GB = LB + lw * GR;
            const i32x4 rx = mk_rsrc(x, 2048 * 512 * 2), rw = mk_rsrc(wqkv, 512 * 1536 * 2);
            const u32 vx0 = (u32)(lane >> 3) * 1024 + (((lane & 7) ^ ((lane >> 4) & 3)) << 4), vx1 = vx0 ^ 64;
            auto fill_x = [&](int bk) {
                u32 so = (u32)(rb * 64) * 1024 + (u32)((bk + rot) & 3) * 256 + lw * 128, ld = GB + bk * SL;
#pragma unroll
                for (int j = 0; j < 8; j++) { dma16(ld, (j & 1) ? vx1 : vx0, rx, so); ld += 1024; so += 8 * 1024; }
            };
            const u32 vw = (u32)(lane >> 3) * 3072 + (((lane & 7) ^ (((lane >> 4) & 1) << 2)) << 4);
            auto fill = [&](int f) {
                const int seg = (f >> 2) == 0 ? 1 : (f >> 2) == 1 ? 2 : 0;
                u32 so = ((u32)((f + rot) & 3) * 128 + lw * 64) * 3072 + seg * 1024 + hq * 128;
                u32 ld = GB + fill_slot(f) * SL;
#pragma unroll
                for (int j = 0; j < 8; j++) { dma16(ld, vw, rw, so); ld += 1024; so += 8 * 3072; }
            };
            fill_x(0); fill(0); fill_x(1); fill(1);
            WAITV(16); STAMP(1); RBAR();
            fill_x(2); fill(2);
            WAITV(16); RBAR();
            fill_x(3); fill(3);
            WAITV(16); RBAR();
            fill(4); fill(5);
            WAITV(16); STAMP(2); RBAR();
            fill(6);
            WAITV(16); RBAR();
            fill(7); prep();
            WAITV(16); RBAR();
            fill(8);
            WAITV(16); RBAR();
            fill(9);
            WAITV(16); STAMP(3); RBAR();
            fill(10);
            {
                const i32x4 rf = mk_rsrc(lw ? wu : wg, b < 224 ? 512 * 1792 * 2 : 0);
                const u32 vf_ = (u32)(lane >> 3) * 3584 + (((lane & 7) ^ (((lane >> 4) & 1) << 2)) << 4);
                u32 so = (u32)(b & 7) * 64 * 3584 + (u32)(b >> 3) * 128, ld = GB;
#pragma unroll
                for (int j = 0; j < 8; j++) { dma16(ld, vf_, rf, so); ld += 1024; so += 8 * 3584; }
            }
            WAITV(24); RBAR();
            fill(11);
            WAITV(24); RBAR();
            fill_o(lw * 8 + 4, 0, 8);
            WAITV(8); STAMP(4); lds_sig(flg + F_LAND * 4);
            fill_o(lw * 8 + 1, 0, 8); WAITV(8); fill_o(lw * 8 + 5, 0, 8);
            __builtin_amdgcn_s_setprio(0);
        } else {
            const u32x4 o4 = {ONES, ONES, ONES, ONES};
            *(LAS u32*)(LB + L_FLG + (tid - 128) * 4) = 0u;
            if (wv == 2) {
                const u32 ep_v = __hip_atomic_load((const GAS u32*)(ws + WS_EPOCH), RLX_AGENT); *(volatile LAS u32*)(flg + F_TAG * 4) = ep_v + 1u;
                lds_w128(LB + L_ONES + l16, o4); st_sc1(bws, WS_ONES + b * 1024 + l16, o4);
                prep();
                BAR();
#pragma unroll
                for (int i = 1; i < 9; i++) RBAR();
                WAITV(0); RBAR();
            } else {
                const int rrow = rb * 64 + hq * 8 + (lane >> 3);
                u32x4 xr[8], wr_[8];
#pragma unroll
                for (int i = 0; i < 8; i++) { xr[i] = *(const GAS u32x4*)(x + (size_t)rrow * 512 + i * 64 + (lane & 7) * 8); wr_[i] = *(const GAS u32x4*)(wr + i * 64 + (lane & 7) * 8); }
                prep();
                BAR();
                tag = __builtin_amdgcn_readfirstlane(*(volatile LAS u32*)(flg + F_TAG * 4));
                float a0 = 0, a1 = 0, a2 = 0, a3 = 0;
#pragma unroll
                for (int i = 0; i < 8; i++) {
#pragma unroll
                    for (int e = 0; e < 4; e++) { a0 = dot2(xr[i][e], ONES, a0); a1 = dot2(xr[i][e], xr[i][e], a1); a2 = dot2(xr[i][e], wr_[i][e], a2); a3 = dot2(wr_[i][e], ONES, a3); }
                    if (i == 3) { asm volatile("" : "+v"(a0), "+v"(a1), "+v"(a2), "+v"(a3)); RBAR(); }
                }
                asm volatile("" : "+v"(a0), "+v"(a1), "+v"(a2), "+v"(a3));
                RBAR();
                {
                    const float mean = red8(a0) * (1.0f / 512.0f), var = fmaxf(red8(a1) * (1.0f / 512.0f) - mean * mean, 0.f);
                    const float m = rbf(mean), sd = rbf(__builtin_amdgcn_sqrtf(rbf(rbf(var) + 1e-5f)));
                    const u32 rbits = pk_bf16((red8(a2) - m * red8(a3)) * __builtin_amdgcn_rcpf(sd), 0.f) & 0xffffu;
                    const u32 key = (((rbits & 0x8000u) ? (~rbits & 0xffffu) : (rbits | 0x8000u)) << 11) | (u32)(2047 - rrow);
                    if ((lane & 7) < 2) __builtin_amdgcn_raw_buffer_store_b64((u32x2){(lane & 1) ? pk_bf16(m, sd) : key, tag}, bws, ((lane & 1) ? WS_S : WS_R) + rrow * 8, 0, 16);
                }
#pragma unroll
                for (int i = 3; i < 8; i++) RBAR();
                u32x4 kq[16];
#pragma unroll
                for (int i = 0; i < 16; i++) kq[i] = __builtin_amdgcn_raw_buffer_load_b128(bws, l16 + i * 1024, WS_R, 16);
                RBAR(); RBAR();
                for (;;) {
                    u32 d = 0;
#pragma unroll
                    for (int i = 0; i < 16; i++) d |= (kq[i].y ^ tag) | (kq[i].w ^ tag);
                    if (__all(d == 0u) || ++spins > SPIN_CAP) break;
#pragma unroll
                    for (int i = 0; i < 16; i++) kq[i] = __builtin_amdgcn_raw_buffer_load_b128(bws, l16 + i * 1024, WS_R, 16);
                    asm volatile("" ::: "memory");
                }
                STAMP(7);
                u32 lim = 0xffffffffu, m1 = 0xffffffffu, m2 = 0, sel = 0;
#pragma unroll 1
                for (int r = 0; r < 8; r++) {
                    if (__any(m1 == 0xffffffffu)) {
                        m1 = 0; m2 = 0;
#pragma unroll
                        for (int i = 0; i < 32; i++) { u32 k = kq[i >> 1][(i & 1) * 2]; if (r) k = k < lim ? k : 0u;
                            const u32 lo = k < m1 ? k : m1; m2 = m2 > lo ? m2 : lo; m1 = m1 > k ? m1 : k; }
                    }
                    lim = wave_umax(m1);
                    if (m1 == lim) { m1 = m2; m2 = 0xffffffffu; }
                    if ((lane & 7) == r) sel = lim;
                }
                if (lane < 8) *(LAS u32*)(LB + L_SEL + lane * 4) = sel;
                lds_sig(flg + F_SEL * 4);
                STAMP(8);
                if (b < 224) {
                    const int srow = 2047 - (int)(sel & 2047u);
                    const u16* xp = x + (size_t)srow * 512 + (b & 7) * 64 + (lane >> 4) * 8;
                    u32x4 xs0 = *(const GAS u32x4*)xp, xs1 = *(const GAS u32x4*)(xp + 32);
                    u32x2 st;
                    do { st = __builtin_amdgcn_raw_buffer_load_b64(bws, WS_S + srow * 8, 0, 16); asm volatile("" ::: "memory"); } while (!__all(st.y == tag) && ++spins <= SPIN_CAP);
                    const float m = bf_lo(st.x), rs = __builtin_amdgcn_rcpf(bf_hi(st.x));
#pragma unroll
                    for (int e = 0; e < 4; e++) { xs0[e] = norm2(xs0[e], m, rs); xs1[e] = norm2(xs1[e], m, rs); }
                    lds_cnt(flg + F_LAND * 4, 2);
                    const u32 wa = LB + (u32)(8 * (lane >> 4) + ((lane & 15) >> 2)) * 128 + ((lane & 3) << 3), sw = ((lane >> 3) & 1) << 6;
                    const u32 go = WS_GU + (u32)b * 4096 + ((lane >> 4) * 32 + (lane & 7)) * 8;
#pragma unroll
                    for (int ct = 0; ct < 4; ct++) {
                        f32x4 d[2];
#pragma unroll
                        for (int mt = 0; mt < 2; mt++) {
                            const u32 a = wa + mt * GR + ((((ct >> 1) << 6) ^ sw) + (ct & 1) * 32);
                            d[mt] = mfma16(cat(tr_read(a), tr_read(a + 512)), xs0, bc4(0.f));
                            d[mt] = mfma16(cat(tr_read(a + 4096), tr_read(a + 4096 + 512)), xs1, d[mt]);
                        }
                        if ((lane & 8) == 0) {
#pragma unroll
                            for (int e = 0; e < 4; e++) __builtin_amdgcn_raw_buffer_store_b64((u32x2){__float_as_uint(d[0][e]), __float_as_uint(d[1][e])}, bws, go + (ct * 16 + e) * 64, 0, 16);
                        }
                    }
                    WAITV(0); st_flag(WS_FGU + b * FS);
                } else lds_cnt(flg + F_LAND * 4, 2);
                STAMP(9);
            }
        }
        tag = __builtin_amdgcn_readfirstlane(*(volatile LAS u32*)(flg + F_TAG * 4));
        if (wv == 2) {
            const int gg = rb * 2 - 8 + (lane & 7);
            const GAS u32* fp = (const GAS u32*)(ws + ((lane & 16) ? WS_FLAGV : WS_FLAGK) + ((gg < 0 ? 0 : gg) * 16 + hq * 2 + ((lane >> 3) & 1)) * FS);
            for (;;) {
                const u32 ok = (u32)__ballot(gg < 0 || __hip_atomic_load(fp, RLX_AGENT) == tag);
                const bool done = ok == 0xffffffffu || ++spins > SPIN_CAP;
                if (lane == 0) *(volatile LAS u32*)(flg + F_HALO * 4) = done ? 0xffffffffu : ok;
                if (done) break;
            }
        }
        lds_bits(flg + F_HALO * 4, kbits);
        STAMP(5);
        ld_k();
        lds_bits(flg + F_HALO * 4, vbits);
        ld_v();
        STAMP(6);
    } else {
    __builtin_amdgcn_s_setprio(2);
    u32 xfa[2][4], wfa[2][4];
    {
        const int row = rg * 32 + l31;
        const u32 w0 = LB + (u32)(8 * h + ((lane & 15) >> 2)) * 128 + ((pr ^ ((lane >> 3) & 1)) << 6) + (((lane >> 4) & 1) << 5) + ((lane & 3) << 3);
#pragma unroll
        for (int s = 0; s < 4; s++) {
            xfa[0][s] = opaque(LB + (row >> 3) * 1024 + (row & 7) * 128 + (((2 * (s ^ cw) + h) ^ ((row >> 1) & 7)) << 4)); xfa[1][s] = opaque(xfa[0][s] + GR);
            wfa[0][s] = opaque(w0 + (s ^ cw) * 2048); wfa[1][s] = opaque(w0 + (s ^ cw) * 2048 + GR);
        }
    }
    const u32 csw = opaque(LB + L_CS + (rg * 64 + pr * 32 + ((lane & 1) << 4) + (lane >> 4) * 4) * 4), csr = opaque(LB + L_CS + (h * 64 + pr * 32 + l31) * 4);
    prep();
    const u32 selv = opaque((((lane >> 4) ^ lane) & 1) ? 0u : ONES);
    const u32x4 sel = {selv, selv, selv, selv};
    u32 blk = (u32)(g * 32 + pair * 2) * 1024, fo = (u32)(g * 16 + pair) * FS;
    asm volatile("" : "+s"(blk), "+s"(fo));
    const u32 vst = opaque(((lane >> 4) & 1) * 1024 + vlo);
    const u32 sta = opaque(LB + L_STP + (rg * 32 + l31) * 16);
    u32x4 xf[8][4], wf[2][4];
    f32x16 acc2;
    f32x4 cs;
    float sx0 = 0, sx1 = 0, sq0 = 0, sq1 = 0;
    auto rw_ = [&](int n, int s) {
        const u32 a = wfa[n & 1][s] + fill_slot(n >> 1) * SL;
        const u32x2 lo = tr_read(a), hi = tr_read(a + 512); wf[n & 1][s] = (u32x4){lo.x, lo.y, hi.x, hi.y};
        if (n < 8) xf[n][s] = lds_r128(xfa[n & 1][s] + (n >> 1) * SL);
    };
    auto mm_ = [&](int n, int s, f32x16& a) {
        const f32x16 c = ((n & 7) == 0 && s == 0) ? zero : a;
        a = (n >> 3) != 1 ? mfma32(wf[n & 1][s], xf[n & 7][s], c) : mfma32(xf[n & 7][s], wf[n & 1][s], c);
        PIN4(a);
    };
    auto cs_ = [&](int n, int s) { cs = mfma16(wf[n & 1][s], sel, ((n & 7) == 0 && s == 0) ? bc4(0.f) : cs); PIN4(cs); };
    auto st_ = [&](int i) {
#pragma unroll
        for (int s = 0; s < 2; s++) { const u32x4 v = xf[i][s];
            sx0 = dot2(v.x, ONES, sx0); sq0 = dot2(v.x, v.x, sq0); sx1 = dot2(v.y, ONES, sx1); sq1 = dot2(v.y, v.y, sq1);
            sx0 = dot2(v.z, ONES, sx0); sq0 = dot2(v.z, v.z, sq0); sx1 = dot2(v.w, ONES, sx1); sq1 = dot2(v.w, v.w, sq1); }
        asm volatile("" : "+v"(sx0), "+v"(sx1), "+v"(sq0), "+v"(sq1));
    };
#define RW(n, s) do { if ((n) < 24) rw_(n, s); } while (0)
#define STEP(n, A, X0, X1, X2, X3) do { STAMPS_STEP(n); \
        mm_(n, 0, A); SB(); RW(n + 1, 0); X0; SB(); cs_(n, 0); SB(); \
        mm_(n, 1, A); SB(); RW(n + 1, 1); X1; SB(); \
        mm_(n, 2, A); SB(); RW(n + 1, 2); X2; SB(); cs_(n, 2); SB(); \
        mm_(n, 3, A); SB(); RW(n + 1, 3); X3; SB(); if ((n) < 8) st_(n); if (!((n) & 1) && (n) <= 16) { if ((n) == 8 || (n) == 16) asm volatile("s_waitcnt lgkmcnt(8)" ::: "memory"); RBAR(); } if ((n) == 18) lds_cnt(flg + F_LAND * 4, 2); SB(); } while (0)
#define NOP_ do {} while (0)
#define STEP0(n, A) STEP(n, A, NOP_, NOP_, NOP_, NOP_)
    STAMP(1);
    RBAR();
    STAMP(2);
#pragma unroll
    for (int s = 0; s < 4; s++) rw_(0, s);
    tag = __builtin_amdgcn_readfirstlane(*(volatile LAS u32*)(flg + F_TAG * 4));
    SB();
    STEP0(0, acc); STEP0(1, acc); STEP0(2, acc); STEP0(3, acc); STEP0(4, acc); STEP0(5, acc); STEP0(6, acc); STEP0(7, acc);
    STAMP(5);
    {
        auto sw = __builtin_amdgcn_permlane32_swap(__float_as_uint(sx0 + sx1), __float_as_uint(sq0 + sq1), false, false);
        lds_wf(sta + h * 8 + pr * 4, __uint_as_float(sw[0]) + __uint_as_float(sw[1]));
        lds_wf4(csw, cs);
    }
    SB();
    float rs_t;
    u32x4 mfr;
    u32x4 kA, kB;
    {
        f32x4 sp; float mean, var, sd, c; u32 mp; u32x4 cf; u32x2 k0, k1;
        STEP0(8, acc2);
        sp = lds_rf4(sta); c = lds_rf(csr); SB();
        STEP(9, acc2, (mean = np(np(sp.x + sp.y) * (-1.0f / 512.0f)), mp = pk_bf16(mean, mean), mfr = (u32x4){mp, mp & 0xffffu, 0u, 0u}), (cf = split3(c)),
             (acc = mfma32(cf, mfr, acc), var = np(np(sp.z + sp.w) * (1.0f / 512.0f)), var = rbf(fmaxf(var - mean * mean, 0.f))),
             (sd = rbf(__builtin_amdgcn_sqrtf(rbf(var + 1e-5f))), rs_t = __builtin_amdgcn_rcpf(sd)));
        STEP(10, acc2, (k0 = scl4(Q4(acc, 0), bc4(rs_t))), (k1 = scl4(Q4(acc, 1), bc4(rs_t)), kA = cat(k0, k1), st_sc1(bws, WS_K + blk + l16, kA)), (k0 = scl4(Q4(acc, 2), bc4(rs_t))),
             (k1 = scl4(Q4(acc, 3), bc4(rs_t)), kB = cat(k0, k1), st_sc1(bws, WS_K + 1024 + blk + l16, kB)));
        STAMP(6);
        STEP(11, acc2, (lds_wf(LB + L_ST + (rg * 32 + l31) * 4 + h * 256, h ? -bf_lo(mp) : rs_t)), lds_w128(own + l16, kA), NOP_, lds_w128(own + 1024 + l16, kB));
    }
    STEP0(12, acc2); STEP0(13, acc2);
    STEP(14, acc2, NOP_, NOP_, NOP_, (WAITV(0), st_flag(WS_FLAGK + fo)));
    STAMP(7);
    STEP0(15, acc2);
    lds_wf4(csw + 512, cs);
    SB();
    {
        const u32 sa = LB + L_ST + (rg * 32 + 4 * h) * 4;
        f32x4 r0, r1, r2, r3; float c; u32x4 cf, vS; u32x2 v0, v1;
        STEP0(16, acc);
        c = lds_rf(csr + 512); SB();
        STEP(17, acc, (cf = split3(c)), (acc2 = mfma32(mfr, cf, acc2), r0 = lds_rf4(sa), r1 = lds_rf4(sa + 32)), (r2 = lds_rf4(sa + 64), r3 = lds_rf4(sa + 96)), (v0 = scl4(Q4(acc2, 0), r0)));
        STEP(18, acc, (v1 = scl4(Q4(acc2, 1), r1)), (vS = cat(v0, v1), st_sc1(bws, WS_V + blk + vst, vS), lds_w128(own + 2048 + vst, vS)), (v0 = scl4(Q4(acc2, 2), r2)),
             (v1 = scl4(Q4(acc2, 3), r3), vS = cat(v0, v1), st_sc1(bws, WS_V + 512 + blk + vst, vS), lds_w128(own + 2048 + 512 + vst, vS)));
    }
    STAMP(8);
    STEP0(19, acc); STEP0(20, acc); STEP0(21, acc);
    STEP(22, acc, NOP_, NOP_, NOP_, (WAITV(0), st_flag(WS_FLAGV + fo)));
    STAMP(9);
    {
        cs_(23, 0); cs_(23, 2); SB();
        mm_(23, 0, acc); SB(); lds_wf4(csw + 1024, cs); lds_sig(flg + F_CS * 4); SB();
        mm_(23, 1, acc); SB(); lds_bits(flg + F_HALO * 4, kbits); ld_k(); SB();
        STAMP(10);
        mm_(23, 2, acc); SB(); lds_cnt(flg + F_CS * 4, 4); const float c = lds_rf(csr + 1024); SB();
        mm_(23, 3, acc); SB();
        acc = mfma32(split3(c), mfr, acc);
        const f32x4 sF = bc4(rs_t * (0.25f * 1.4426950408889634f));
        const u32x4 qA = cat(scl4(Q4(acc, 0), sF), scl4(Q4(acc, 1), sF)), qB = cat(scl4(Q4(acc, 2), sF), scl4(Q4(acc, 3), sF));
        lds_w128(LB + L_Q + cw * 2048 + l16, qA); lds_w128(LB + L_Q + cw * 2048 + 1024 + l16, qB);
        float m = dot2(qA.x, kA.x, 0.f), m2 = dot2(qB.x, kB.x, 0.f);
        m = dot2(qA.y, kA.y, m); m2 = dot2(qB.y, kB.y, m2); m = dot2(qA.z, kA.z, m); m2 = dot2(qB.z, kB.z, m2); m = dot2(qA.w, kA.w, m); m2 = dot2(qB.w, kB.w, m2);
        auto sw = __builtin_amdgcn_permlane32_swap(__float_as_uint(m), __float_as_uint(m2), false, false);
        lds_wf(LB + L_M + cw * 256 + lane * 4, -(__uint_as_float(sw[0]) + __uint_as_float(sw[1])));
    }
    lds_sig(flg + F_Q * 4);
    STAMP(11);
    }
    __builtin_amdgcn_s_setprio(1);
    lds_cnt(flg + F_Q * 4, 4);

    STAMP(12);
    const u32x4 qF = lds_r128(LB + L_Q + cw * 2048 + hd * 1024 + l16), kO = lds_r128(own + hd * 1024 + l16);
    const float mF = lds_rf(LB + L_M + cw * 256 + hd * 128 + l31 * 4);
    const u32 ownv = (lane & 16) ? LB + L_ONES : own + 2048 + hd * 1024 + vlo;
    u32x4 vX0 = lds_r128(ownv), vX1 = lds_r128(ownv + 512), kX = kO;
    const u32x4 qS = lds_r128(LB + L_Q + (cw ^ 1) * 2048 + hd * 1024 + l16);
    const float mS = lds_rf(LB + L_M + (cw ^ 1) * 256 + hd * 128 + l31 * 4);
    f32x16 cF, cS, oF, oS, sa, sb;
#pragma unroll
    for (int i = 0; i < 16; i++) cF[i] = mF;
    sa = mfp(kO, qF, cF); SB();
#pragma unroll
    for (int i = 0; i < 16; i++) cS[i] = mS;
    PIN4(cS); SB();
    u32x4 pa[2], pb[2], pc[2], pd[2];
#define EX2(S, i) pkp(__builtin_amdgcn_exp2f(S[2 * (i)]), __builtin_amdgcn_exp2f(S[2 * (i) + 1]))
#define TILE(S, MASK, P, M0, M1, M2, M3) do { \
        P[0][0] = EX2(S, 0); P[0][1] = EX2(S, 1); SB(); M0; SB(); P[0][2] = EX2(S, 2); P[0][3] = EX2(S, 3); \
        if (MASK) { _Pragma("unroll") for (int i = 0; i < 4; i++) P[0][i] = opaque(P[0][i] & dmask[i]); } SB(); M1; SB(); \
        P[1][0] = EX2(S, 4); P[1][1] = EX2(S, 5); SB(); M2; SB(); P[1][2] = EX2(S, 6); P[1][3] = EX2(S, 7); \
        if (MASK) { _Pragma("unroll") for (int i = 0; i < 4; i++) P[1][i] = opaque(P[1][i] & dmask[4 + i]); } SB(); M3; SB(); } while (0)
    TILE(sa, true, pa, NOP_, (oF = mfp(vX0, pa[0], zero)), (sb = mfp(kf[2], qS, cS)), (oF = mfp(vX1, pa[1], oF)));
    STAMP(13);
    asm volatile("" : "+v"(kf[0]), "+v"(kf[1]), "+v"(kf[3]));
    if (!hd) { lds_bits(flg + F_HALO * 4, vbits); ld_v(); }
    const int erow = (hq & 1) * 32 + (tid >> 4);
    const int orow = rb * 64 + erow, ocol = (hq >> 1) * 128 + (tid & 15) * 8;
    u32x4 xe = *(const GAS u32x4*)(x + (size_t)orow * 512 + ocol);
    STAMP(14);
    {
        const u32 om = rg ? 0xffffffffu : 0u;
#pragma unroll
        for (int i = 0; i < 8; i++) dmask[i] = opaque(~dmask[i] | om);
        if (rg) kX = kf[0];
        PIN4(kX);
    }
    SB();
    TILE(sb, false, pa, (sa = mfp(kf[3], qS, cS)), NOP_, NOP_, (sb = mfp(kf[1], qS, cS)));
    TILE(sa, false, pb, NOP_, NOP_, NOP_, (sa = mfp(kX, qS, cS)));
    STAMP(15);
    TILE(sb, true, pc, (oS = mfp(vf[2][0], pa[0], zero)), (oS = mfp(vf[2][1], pa[1], oS)), (oS = mfp(vf[3][0], pb[0], oS)), (sb = mfp(kf[0], qF, cF)));
    if (rg) { vX0 = vf[0][0]; vX1 = vf[0][1]; }
    PIN4(vX0); PIN4(vX1);
    asm volatile("" : "+v"(vf[0][0]), "+v"(vf[0][1]), "+v"(vf[1][0]), "+v"(vf[1][1]), "+v"(xe));
    STAMP(16);
    const int osl = (wv >> 1) * 8 + (wv & 1) * 4 + 2;
#define WO(j) do { if (hd) fill_o(osl + ((j) >> 1), ((j) & 1) * 4, ((j) & 1) * 4 + 4); SB(); } while (0)
    WO(0);
    if (wv == 3) { fill_o(0, 0, 8); fill_o(8, 0, 8); }
    u32x4 wdr[8];
    if (wv == 0 && b < 224) {
#pragma unroll
        for (int i = 0; i < 8; i++) wdr[i] = *(const GAS u32x4*)(wd + (size_t)((b >> 3) * 64 + i * 8 + (lane >> 3)) * 512 + (b & 7) * 64 + (((lane & 7) ^ (((lane >> 4) & 1) << 2)) << 3));
    }
    SB();
    TILE(sa, false, pd, (oS = mfp(vf[3][1], pb[1], oS)), (oS = mfp(vf[1][0], pc[0], oS)), (oS = mfp(vf[1][1], pc[1], oS)), (sa = mfp(kf[1], qF, cF)));
    WO(1);
    TILE(sb, true, pa, (oS = mfp(vX0, pd[0], oS)), (oS = mfp(vX1, pd[1], oS)), NOP_, (sb = mfp(kf[2], qF, cF)));
    WO(2);
    const u32 oxw = LB + L_OWN + cw * 4096 + hd * 1024;
    TILE(sa, false, pb, (oF = mfp(vf[0][0], pa[0], oF)), (lds_wf4(oxw + l16, Q4(oS, 0)), lds_wf4(oxw + 2048 + l16, Q4(oS, 1)), lds_wf(LB + L_L + wv * 256 + lane * 4, oS[8]), lds_sig(flg + (F_O + wv) * 4)),
         (oF = mfp(vf[0][1], pa[1], oF)), (sa = mfp(kf[3], qF, cF)));
    STAMP(17);
    WO(3);
    TILE(sb, false, pc, (oF = mfp(vf[1][0], pb[0], oF)), NOP_, (oF = mfp(vf[1][1], pb[1], oF)), (oF = mfp(vf[2][0], pc[0], oF)));
    TILE(sa, false, pd, (oF = mfp(vf[2][1], pc[1], oF)), NOP_, (oF = mfp(vf[3][0], pd[0], oF)), (oF = mfp(vf[3][1], pd[1], oF)));
    STAMP(19);
    lds_cnt(flg + (F_O + (wv ^ 1)) * 4, 1);
    STAMP(20);
    {
        const u32 e = LB + L_OWN + (cw ^ 1) * 4096 + hd * 1024;
        const f32x4 t0 = lds_rf4(e + l16), t1 = lds_rf4(e + 2048 + l16); const float tl = lds_rf(LB + L_L + (wv ^ 1) * 256 + lane * 4);
        float l = oF[8] + tl;
        f32x4 o0 = add4(Q4(oF, 0), t0), o1 = add4(Q4(oF, 1), t1);
        const float amax = fmaxf(fmaxf(fmaxf(fabsf(o0.x), fabsf(o0.y)), fmaxf(fabsf(o0.z), fabsf(o0.w))), fmaxf(fmaxf(fabsf(o1.x), fabsf(o1.y)), fmaxf(fabsf(o1.z), fabsf(o1.w))));
        if (__builtin_expect(FORCE_FB || !__all(l >= 0x1p-60f && l <= 0x1p60f && amax <= 0x1p120f && o0.x + o0.y + o0.z + o0.w + o1.x + o1.y + o1.z + o1.w == o0.x + o0.y + o0.z + o0.w + o1.x + o1.y + o1.z + o1.w), 0)) {
            const int j0 = g >= 8 ? g - 8 : 0, head = pair * 2 + hd;
            { int gg = j0 + (lane & 15); gg = gg > g ? g : gg;
              const GAS u32* fp = (const GAS u32*)(ws + ((lane & 16) ? WS_FLAGV : WS_FLAGK) + (gg * 16 + pair) * FS);
              while (!__all(__hip_atomic_load(fp, RLX_AGENT) == tag) && ++spins <= SPIN_CAP) {}
              __builtin_amdgcn_fence(__ATOMIC_ACQUIRE, "wavefront"); }
            float mx = -3.0e38f;
            f32x16 o = zero;
#pragma unroll 1
            for (int pass = 0; pass < 2; pass++) {
#pragma unroll 1
                for (int j = j0; j <= g; j++) {
                    const u32 bo = (u32)(j * 32 + head) * 1024;
                    const f32x16 sc = mfma32(__builtin_amdgcn_raw_buffer_load_b128(bws, l16, WS_K + bo, 16), qF, zero);
                    const int dq = (g - j) * 32 + l31 - 4 * h;
                    float p[16];
#pragma unroll
                    for (int i = 0; i < 16; i++) { const int d = dq - (i & 3) - 8 * (i >> 2); const bool vis = d >= 0 && d < 256;
                        if (vis) mx = pass ? mx : fmaxf(mx, sc[i]); p[i] = vis ? __builtin_amdgcn_exp2f(sc[i] - mx) : 0.f; }
                    if (pass) {
                        const u32 va = (lane & 16) ? WS_ONES + b * 1024 : WS_V + vlo + bo;
                        o = mfma32(__builtin_amdgcn_raw_buffer_load_b128(bws, va, 0, 16), (u32x4){pk_bf16(p[0], p[1]), pk_bf16(p[2], p[3]), pk_bf16(p[4], p[5]), pk_bf16(p[6], p[7])}, o);
                        o = mfma32(__builtin_amdgcn_raw_buffer_load_b128(bws, va + 512, 0, 16), (u32x4){pk_bf16(p[8], p[9]), pk_bf16(p[10], p[11]), pk_bf16(p[12], p[13]), pk_bf16(p[14], p[15])}, o);
                    }
                }
                auto sw = __builtin_amdgcn_permlane32_swap(__float_as_uint(mx), __float_as_uint(mx), false, false);
                mx = fmaxf(__uint_as_float(sw[0]), __uint_as_float(sw[1]));
            }
            l = o[8]; o0 = Q4(o, 0); o1 = Q4(o, 1);
        }
        const float inv = __builtin_amdgcn_rcpf(l);
        st_sc1(bws, WS_O + (u32)(g * 32 + pair * 2 + hd) * 1024 + l16, cat(scl4(o0, bc4(inv)), scl4(o1, bc4(inv))));
    }
    STAMP(21);
    __builtin_amdgcn_s_setprio(0);
    SB();
    u32 gp[4], xnp[4];
    {
        const float e_m = lds_rf(LB + L_ST + 256 + erow * 4), e_rs = lds_rf(LB + L_ST + erow * 4);
#pragma unroll
        for (int e = 0; e < 4; e++) { xnp[e] = norm2(xe[e], e_m, e_rs); gp[e] = opaque(pk_bf16(silu_(bf_lo(xnp[e])), silu_(bf_hi(xnp[e])))); }
    }
    SB();
    WAITV(0); st_flag(WS_FLAGO + (u32)(g * 32 + pair * 2 + hd) * FS);
    STAMP(22);
    SB();
    if (b < 224) {
        const GAS u32* fg = (const GAS u32*)(ws + WS_FGU + ((b & ~7) + (lane & 7)) * FS);
        while (!__all(__hip_atomic_load(fg, RLX_AGENT) == tag) && ++spins <= SPIN_CAP) {}
        __builtin_amdgcn_fence(__ATOMIC_ACQUIRE, "wavefront");
        u32x2 t[8];
#pragma unroll
        for (int i = 0; i < 8; i++) t[i] = __builtin_amdgcn_raw_buffer_load_b64(bws, lane * 8 + wv * 512, WS_GU + (u32)((b & ~7) + i) * 4096, 16);
        float ga = 0, ua = 0;
#pragma unroll
        for (int i = 0; i < 8; i++) { ga += __uint_as_float(t[i].x); ua += __uint_as_float(t[i].y); }
        const float hv = rbf(silu_(rbf(ga))) * rbf(ua);
        *(LAS u16*)(LB + L_H + (lane & 7) * 128 + (wv * 8 + (lane >> 3)) * 2) = (u16)pk_bf16(hv, hv);
    }
    STAMP(24);
    BAR();
    STAMP(23);
    const int og = rb * 2 + (hq & 1), kh = wv >> 2;
    if (wv == 0 && b < 224) {
#pragma unroll
        for (int i = 0; i < 8; i++) lds_w128(LB + L_Q + i * 1024 + l16, wdr[i]);
        const u32x4 h0 = lds_r128(LB + L_H + (lane & 7) * 128 + (lane >> 4) * 16), h1 = lds_r128(LB + L_H + (lane & 7) * 128 + 64 + (lane >> 4) * 16);
        const u32 wa = LB + L_Q + (u32)(8 * (lane >> 4) + ((lane & 15) >> 2)) * 128 + ((lane & 3) << 3), sw = ((lane >> 3) & 1) << 6;
        const u32 yo = WS_Y + (u32)(((lane & 7) * 8 + (b & 7)) * 28 + (b >> 3)) * 512 + (lane >> 4) * 32;
#pragma unroll
        for (int ct = 0; ct < 4; ct++) {
            const u32 a = wa + ((((ct >> 1) << 6) ^ sw) + (ct & 1) * 32);
            f32x4 d = mfma16(cat(tr_read(a), tr_read(a + 512)), h0, bc4(0.f));
            d = mfma16(cat(tr_read(a + 4096), tr_read(a + 4096 + 512)), h1, d);
            const u32x4 v = (lane & 8) ? (u32x4){__float_as_uint(d.z), tag, __float_as_uint(d.w), tag} : (u32x4){__float_as_uint(d.x), tag, __float_as_uint(d.y), tag};
            st_sc1(bws, yo + ct * 128 + (lane & 8) * 2, v);
        }
    }
    const GAS u32* fq = (const GAS u32*)(ws + WS_FLAGO + (og * 32 + wv * 4 + (lane & 3)) * FS);
    u32 pv = __hip_atomic_load(fq, RLX_AGENT);
    u32x4 wof[16];
    {
        const int q = (lane & 15) >> 2;
        const u32 a = LB + kh * GR + (u32)(4 * h + q) * 256 + ((cw ^ q) << 6) + (((lane >> 4) & 1) << 5) + ((lane & 3) << 3);
#pragma unroll
        for (int s = 0; s < 16; s++) { const u32x2 lo = tr_read(a + s * 4096), hi = tr_read(a + s * 4096 + 2048); wof[s] = (u32x4){lo.x, lo.y, hi.x, hi.y}; }
        SB();
#pragma unroll
        for (int s = 0; s < 16; s++) PIN4(wof[s]);
    }
    const u32 selk = *(LAS u32*)(LB + L_SEL + (lane & 7) * 4);
    const u32 mine = (u32)__ballot(((2047u - (selk & 2047u)) >> 5) == (u32)og) & 0xffu;
    STAMP(25);
    {
        while (!__all(pv == tag) && ++spins <= SPIN_CAP) pv = __hip_atomic_load(fq, RLX_AGENT);
        STAMP(26);
        __builtin_amdgcn_fence(__ATOMIC_ACQUIRE, "wavefront");
        u32x4 t[4];
#pragma unroll
        for (int i = 0; i < 4; i++) t[i] = __builtin_amdgcn_raw_buffer_load_b128(bws, l16 + i * 1024, WS_O + (u32)(og * 32 + wv * 4) * 1024, 16);
        RBAR();
#pragma unroll
        for (int i = 0; i < 4; i++) lds_w128(LB + L_OST + ((wv * 4 + i) ^ ((rb & 7) * 2)) * 1024 + l16, t[i]);
    }
    STAMP(27);
    SB(); BAR();
    STAMP(28);
    constexpr u32 TP = 528;
    {
        u32x4 of[16];
#pragma unroll
        for (int s = 0; s < 16; s++) of[s] = lds_r128(LB + L_OST + (kh * 16 + s) * 1024 + l16);
        SB();
#pragma unroll
        for (int s = 0; s < 16; s++) acc = mfma32(wof[s], of[s], s ? acc : zero);
        const u32 a = LB + L_TR + kh * 32 * TP + l31 * TP + cw * 128 + h * 16;
#pragma unroll
        for (int q = 0; q < 4; q++) lds_wf4(a + q * 32, Q4(acc, q));
    }
    for (u32 mm = mine; mm; mm &= mm - 1) {
        const int tk = __builtin_ctz(mm);
        const u32 yo = WS_Y + (u32)((tk * 8 + (hq >> 1) * 2 + (tid >> 8)) * 28 + ((tid >> 5) & 7)) * 512 + (tid & 31) * 16;
        const bool last = ((tid >> 5) & 7) < 4;
        u32x4 t[4];
        for (;;) {
#pragma unroll
            for (int i = 0; i < 4; i++) t[i] = __builtin_amdgcn_raw_buffer_load_b128(bws, (i < 3 || last) ? yo + i * 4096 : yo, 0, 16);
            u32 d = 0;
#pragma unroll
            for (int i = 0; i < 4; i++) d |= (t[i].y ^ tag) | (t[i].w ^ tag);
            asm volatile("" ::: "memory");
            if (__all(d == 0u) || ++spins > SPIN_CAP) break;
        }
        const float y0 = (__uint_as_float(t[0].x) + __uint_as_float(t[1].x)) + (__uint_as_float(t[2].x) + (last ? __uint_as_float(t[3].x) : 0.f));
        const float y1 = (__uint_as_float(t[0].z) + __uint_as_float(t[1].z)) + (__uint_as_float(t[2].z) + (last ? __uint_as_float(t[3].z) : 0.f));
        *(LAS f32x2*)(LB + L_YR + tk * 4096 + ((tid >> 5) & 7) * 512 + (tid >> 8) * 256 + (tid & 31) * 8) = (f32x2){y0, y1};
    }
    STAMP(29);
    SB(); BAR();
    STAMP(30);
    for (u32 mm = mine; mm; mm &= mm - 1) {
        const int tk = __builtin_ctz(mm);
        const u32 key = __builtin_amdgcn_readlane(selk, tk);
        if (2047 - (int)(key & 2047u) == orow) {
            f32x4 y0 = bc4(0.f), y1 = bc4(0.f);
#pragma unroll
            for (int c = 0; c < 8; c++) { y0 += lds_rf4(LB + L_YR + tk * 4096 + c * 512 + (tid & 15) * 32); y1 += lds_rf4(LB + L_YR + tk * 4096 + c * 512 + (tid & 15) * 32 + 16); }
            const u32 k16 = key >> 11, rbits = (k16 & 0x8000u) ? (k16 & 0x7fffu) : (~k16 & 0xffffu);
            const float rsel = rbf(__builtin_amdgcn_rcpf(1.0f + __builtin_amdgcn_exp2f(bf_lo(rbits) * -1.4426950408889634f)));
            const u32x4 yp = pack8(y0, y1);
#pragma unroll
            for (int e = 0; e < 4; e++) {
                const u32 yy = pk_bf16(bf_lo(yp[e]) * rsel, bf_hi(yp[e]) * rsel);
                const u32 xb = pk_bf16(bf_lo(xnp[e]) + bf_lo(yy), bf_hi(xnp[e]) + bf_hi(yy));
                gp[e] = pk_bf16(silu_(bf_lo(xb)), silu_(bf_hi(xb)));
            }
        }
    }
    {
        const u32 a = LB + L_TR + (tid >> 4) * TP + (tid & 15) * 32;
        const u32x4 sp4 = pack8(lds_rf4(a) + lds_rf4(a + 32 * TP), lds_rf4(a + 16) + lds_rf4(a + 32 * TP + 16));
        u32x4 ov;
#pragma unroll
        for (int e = 0; e < 4; e++) {
            const u32 t = pk_bf16(bf_lo(sp4[e]) * bf_lo(gp[e]), bf_hi(sp4[e]) * bf_hi(gp[e]));
            ov[e] = pk_bf16(bf_lo(xe[e]) + bf_lo(t), bf_hi(xe[e]) + bf_hi(t));
        }
        *(GAS u32x4*)(out + (size_t)orow * 512 + ocol) = ov;
    }
    if (b == 0 && wv == 3) {
        for (;;) { bool ok = true;
#pragma unroll
            for (int i = 0; i < 4; i++) { const int w = lane + 64 * i; ok &= __hip_atomic_load((const GAS u32*)(ws + WS_FLAGK + ((w >> 3) * 2 * 16 + (w & 7) * 2) * FS), RLX_AGENT) == tag; }
            if (__all(ok) || ++spins > SPIN_CAP) break;
            __builtin_amdgcn_s_sleep(2);
        }
        if (lane == 0) __hip_atomic_store((GAS u32*)(ws + WS_EPOCH), tag, RLX_AGENT);
    }
}
#define STAMP_WAVES 8
#define STAMP_NAMES {"start","pre/L:x0 in","P0 ok/L:Wk in","L:Wv in","L:Wq in","K mfma done/S:Kfl","K stored/S:V iss","flagK/R:gath","V stored/R:sel","flagV/R:A1","K issued","Q done","rel PQ","diag done","V issued","S23 done","S1 done","F1 done","","F done","O ok","o stored","flagO","rel Bw","arr Bw","wof","o poll ok","arr Bo","rel Bo","arr Bs","rel Bs"}

extern "C" void kernel_launch(void* const* d_in, const int* in_sizes, int n_in, void* d_out, int out_size, void* d_ws, size_t ws_size, hipStream_t stream) {
    fused_kernel<<<256, 512, 0, stream>>>((const u16*)d_in[0], (const u16*)d_in[1], (char*)d_ws, (const u16*)d_in[2], (u16*)d_out, (const u16*)d_in[4], (const u16*)d_in[5], (const u16*)d_in[6], (const u16*)d_in[7]);
}
```

```cpp
#include <hip/hip_runtime.h>
#include <stdint.h>

typedef unsigned short u16;
typedef unsigned u32;
typedef unsigned long long u64;
typedef u32 u32x4 __attribute__((ext_vector_type(4)));
typedef u32 u32x2 __attribute__((ext_vector_type(2)));
typedef int i32x4 __attribute__((ext_vector_type(4)));
typedef __bf16 bf16x8 __attribute__((ext_vector_type(8)));
typedef __bf16 bf16x4 __attribute__((ext_vector_type(4)));
typedef __bf16 bf16x2 __attribute__((ext_vector_type(2)));
typedef float f32x16 __attribute__((ext_vector_type(16)));
typedef float f32x4 __attribute__((ext_vector_type(4)));
typedef float f32x2 __attribute__((ext_vector_type(2)));
#define LAS __attribute__((address_space(3)))
#define GAS __attribute__((address_space(1)))
#define DI __device__ __forceinline__
#define RLX_AGENT __ATOMIC_RELAXED, __HIP_MEMORY_SCOPE_AGENT

constexpr u32 WS_EPOCH = 0;
constexpr u32 FS = 64;
constexpr u32 WS_FLAGK = 128 << 10;
constexpr u32 WS_FLAGV = 256 << 10;
constexpr u32 WS_FLAGO = 384 << 10;
constexpr u32 WS_ONES = 512 << 10;
constexpr u32 WS_R = 64 << 10;
constexpr u32 WS_S = 80 << 10;
constexpr u32 WS_FGU = 96 << 10;
constexpr u32 WS_GU = 7 << 20;
constexpr u32 WS_Y = 8 << 20;
constexpr u32 WS_K = 1 << 20;
constexpr u32 WS_V = 3 << 20;
constexpr u32 WS_O = 5 << 20;
constexpr u32 WS_USED = 9 << 20;
#define STAMP(i) do {} while (0)

#define STAMPS_STEP(n) do {} while (0)
constexpr u32 SL = 8192, GR = 8 * SL;
constexpr u32 L_SCR = 2 * GR;
constexpr u32 L_OWN = L_SCR;
constexpr u32 L_Q = L_SCR + 16384;
constexpr u32 L_M = L_SCR + 24576;
constexpr u32 L_STP = L_SCR + 25600;
constexpr u32 L_CS = L_SCR + 26624;
constexpr u32 L_L = L_SCR + 25600;
constexpr u32 L_ST = L_SCR + 28160;
constexpr u32 L_FLG = L_SCR + 28672;
constexpr u32 L_ONES = L_SCR + 29184;
constexpr u32 L_SEL = L_SCR + 30208;
constexpr u32 L_H = L_SCR + 30720;
constexpr u32 L_DEAD = L_SCR + 31744;
constexpr u32 L_YR = 98304;
constexpr u32 L_OST = 65536;
constexpr u32 L_TR = 0;
constexpr u32 L_TOTAL = 163840;
__device__ constexpr u32 fill_slot(int f) { return f < 4 ? 4 + f : ((f - 4) >> 1) + ((f & 1) ? 4 : 0); }
constexpr u32 F_CS = 0;
constexpr u32 F_Q = 1;
constexpr u32 F_LAND = 2;
constexpr u32 F_HALO = 3;
constexpr u32 F_O = 8;
constexpr u32 F_SEL = 16;
constexpr u32 F_TAG = 25;

DI float bf_lo(u32 v) { return __uint_as_float(v << 16); }
DI float bf_hi(u32 v) { return __uint_as_float(v & 0xffff0000u); }
DI u32 pk_bf16(float a, float b) { f32x2 f = {a, b}; return __builtin_bit_cast(u32, __builtin_convertvector(f, bf16x2)); }
DI float rbf(float a) { return bf_lo(pk_bf16(a, a)); }
DI float dot2(u32 a, u32 b, float c) { return __builtin_amdgcn_fdot2_f32_bf16(__builtin_bit_cast(bf16x2, a), __builtin_bit_cast(bf16x2, b), c, false); }
DI f32x16 mfma32(u32x4 a, u32x4 b, f32x16 c) { return __builtin_amdgcn_mfma_f32_32x32x16_bf16(__builtin_bit_cast(bf16x8, a), __builtin_bit_cast(bf16x8, b), c, 0, 0, 0); }
DI f32x4 mfma16(u32x4 a, u32x4 b, f32x4 c) { return __builtin_amdgcn_mfma_f32_16x16x32_bf16(__builtin_bit_cast(bf16x8, a), __builtin_bit_cast(bf16x8, b), c, 0, 0, 0); }
DI u32x2 tr_read(u32 addr) { return __builtin_bit_cast(u32x2, __builtin_amdgcn_ds_read_tr16_b64_v4bf16((LAS bf16x4*)addr)); }
DI u32x4 lds_r128(u32 addr) { return *(LAS u32x4*)addr; }
DI f32x4 lds_rf4(u32 addr) { return *(LAS f32x4*)addr; }
DI void lds_w128(u32 addr, u32x4 v) { *(LAS u32x4*)addr = v; }
DI void lds_wf4(u32 addr, f32x4 v) { *(LAS f32x4*)addr = v; }
template <int CTRL> DI float dpp_add(float v) { return v + __uint_as_float(__builtin_amdgcn_update_dpp(0u, __float_as_uint(v), CTRL, 0xf, 0xf, true)); }
DI float red8(float v) { v = dpp_add<0xB1>(v); v = dpp_add<0x4E>(v); return dpp_add<0x141>(v); }
DI i32x4 mk_rsrc(const void* p, u32 bytes) { u64 a = (u64)p; i32x4 r; r.x = (int)(u32)a; r.y = (int)(u32)(a >> 32) & 0xffff; r.z = (int)bytes; r.w = 0x00020000; return r; }
DI void dma16(u32 lds, u32 voff, i32x4 rsrc, u32 soff) {
    asm volatile("s_mov_b32 m0, %0\n\ts_nop 0\n\tbuffer_load_dwordx4 %1, %2, %3 offen lds" :: "s"(lds), "v"(voff), "s"(rsrc), "s"(soff) : "memory");
}
DI void touch(u32 lds, const GAS void* p) { asm volatile("s_mov_b32 m0, %0\n\ts_nop 0\n\tglobal_load_lds_dword %1, off" :: "s"(lds), "v"(p) : "memory"); }
DI u32 opaque(u32 v) { asm volatile("" : "+v"(v)); return v; }
DI u32x4 pack8(f32x4 a, f32x4 b) { return (u32x4){pk_bf16(a.x, a.y), pk_bf16(a.z, a.w), pk_bf16(b.x, b.y), pk_bf16(b.z, b.w)}; }
#define Q4(acc, q) ((f32x4){acc[4 * (q)], acc[4 * (q) + 1], acc[4 * (q) + 2], acc[4 * (q) + 3]})
#define BAR() asm volatile("s_waitcnt lgkmcnt(0)\n\ts_barrier" ::: "memory")
#define RBAR() asm volatile("s_barrier" ::: "memory")
#define WAITV_(n) asm volatile("s_waitcnt vmcnt(" #n ")" ::: "memory")
#define WAITV(n) ({ WAITV_(n); })
constexpr u32 SPIN_CAP = 1u << 20;
#define XDELAY 6
#define WOEARLY 0
#define FORCE_FB 0
#define WARM 0
#define CODE_LO 0
#define CODE_N 3
#define ROTX 2
#define WOQ 4
constexpr u32 ONES = 0x3f803f80u;

#define SB() __builtin_amdgcn_sched_barrier(0)
DI float np(float r) { asm("" : "+v"(r)); return r; }
DI u32x2 scl4(f32x4 a, f32x4 r) { return (u32x2){pk_bf16(np(a.x * r.x), np(a.y * r.y)), pk_bf16(np(a.z * r.z), np(a.w * r.w))}; }
DI u32x4 split3(float c) {
    const u32 a = pk_bf16(c, c); const float r1 = np(c - bf_lo(a));
    const u32 b = pk_bf16(r1, r1); const float r2 = np(r1 - bf_lo(b));
    return (u32x4){(a & 0xffffu) | (b & 0xffff0000u), pk_bf16(r2, 0.f), 0u, 0u};
}
DI f32x4 addmul4(f32x4 a, f32x4 b, float m) { return (f32x4){np(np(a.x + b.x) * m), np(np(a.y + b.y) * m), np(np(a.z + b.z) * m), np(np(a.w + b.w) * m)}; }
DI f32x4 add4(f32x4 a, f32x4 b) { return (f32x4){np(a.x + b.x), np(a.y + b.y), np(a.z + b.z), np(a.w + b.w)}; }
DI f32x4 bc4(float a) { return (f32x4){a, a, a, a}; }
DI u32x4 cat(u32x2 a, u32x2 b) { return (u32x4){a.x, a.y, b.x, b.y}; }
#define PIN4(v) asm volatile("" : "+v"(v))
DI void lds_sig_(u32& pt, u32 addr) {
    asm volatile("v_mov_b32 %0, %1\n\ts_mov_b64 exec, 1\n\tds_add_u32 %0, %0\n\ts_mov_b64 exec, -1" : "+v"(pt) : "s"(addr) : "memory");
}
DI void lds_need_(u32& pt, u32& have, u32 addr, u32 n) {
    u32 c;
    asm volatile("s_cmp_lt_u32 %0, %4\n\ts_cbranch_scc1 3f\n2:\n\t.subsection 1\n3:\n\ts_mov_b32 %2, 0x400000\n"
                 "1:\n\tv_mov_b32 %1, %3\n\tds_read_b32 %1, %1\n\ts_waitcnt lgkmcnt(0)\n\tv_readfirstlane_b32 %0, %1\n\ts_cmp_ge_u32 %0, %4\n\ts_cbranch_scc1 2b\n\ts_sub_u32 %2, %2, 1\n\ts_cbranch_scc0 1b\n\ts_branch 2b\n\t.subsection 0"
                 : "+s"(have), "+v"(pt), "=&s"(c) : "s"(addr), "s"(n) : "memory", "scc");
}
DI void lds_cnt_(u32& pt, u32 addr, u32 n) {
    u32 c, v;
    asm volatile("s_mov_b32 %2, 0x400000\n"
                 "1:\n\tv_mov_b32 %1, %3\n\tds_read_b32 %1, %1\n\ts_waitcnt lgkmcnt(0)\n\tv_readfirstlane_b32 %0, %1\n\ts_cmp_ge_u32 %0, %4\n\ts_cbranch_scc1 2f\n\ts_sub_u32 %2, %2, 1\n\ts_cbranch_scc0 1b\n2:"
                 : "=&s"(v), "+v"(pt), "=&s"(c) : "s"(addr), "s"(n * addr) : "memory", "scc");
}
DI void lds_bits_(u32& pt, u32 addr, u32 bits) {
    u32 c, v;
    asm volatile("s_mov_b32 %2, 0x400000\n"
                 "1:\n\tv_mov_b32 %1, %3\n\tds_read_b32 %1, %1\n\ts_waitcnt lgkmcnt(0)\n\tv_readfirstlane_b32 %0, %1\n\ts_andn2_b32 %0, %4, %0\n\ts_cbranch_scc0 2f\n\ts_sub_u32 %2, %2, 1\n\ts_cbranch_scc0 1b\n2:"
                 : "=&s"(v), "+v"(pt), "=&s"(c) : "s"(addr), "s"(bits) : "memory", "scc");
}
#define lds_sig(a) lds_sig_(pt, a)
#define lds_need(h, a, n) lds_need_(pt, h, a, n)
#define lds_cnt(a, n) lds_cnt_(pt, a, n)
#define lds_bits(a, b) lds_bits_(pt, a, b)
DI f32x16 mfp(u32x4 a, u32x4 b, f32x16 c) { f32x16 r = mfma32(a, b, c); PIN4(r); return r; }
DI u32 pkp(float a, float b) { return opaque(pk_bf16(a, b)); }
template <int CTRL, int RM> DI u32 dpp_umax(u32 v) { const u32 o = __builtin_amdgcn_update_dpp(0u, v, CTRL, RM, 0xf, false); return v > o ? v : o; }
DI u32 wave_umax(u32 v) { v = dpp_umax<0x111, 0xf>(v); v = dpp_umax<0x112, 0xf>(v); v = dpp_umax<0x114, 0xf>(v); v = dpp_umax<0x118, 0xf>(v); v = dpp_umax<0x142, 0xa>(v); v = dpp_umax<0x143, 0xc>(v);
    return __builtin_amdgcn_readlane(v, 63); }
DI float silu_(float x) { return x * __builtin_amdgcn_rcpf(1.0f + __builtin_amdgcn_exp2f(x * -1.4426950408889634f)); }
DI u32 norm2(u32 x, float m, float rs) { const u32 d = pk_bf16(bf_lo(x) - m, bf_hi(x) - m); return pk_bf16(bf_lo(d) * rs, bf_hi(d) * rs); }
DI void st_sc1(__amdgpu_buffer_rsrc_t r, u32 off, u32x4 v) { __builtin_amdgcn_raw_buffer_store_b128(v, r, off, 0, 16); }
DI float swap_add(float m) { auto sw = __builtin_amdgcn_permlane32_swap(__float_as_uint(m), __float_as_uint(m), false, false); return __uint_as_float(sw[0]) + __uint_as_float(sw[1]); }
DI float lds_rf(u32 a) { return *(LAS float*)a; }
DI void lds_wf(u32 a, float v) { *(LAS float*)a = v; }

__global__ __launch_bounds__(512, 2) void fused_kernel(const u16* __restrict__ x, const u16* __restrict__ wqkv, char* __restrict__ ws, const u16* __restrict__ wo, u16* __restrict__ out,
                                                        const u16* __restrict__ wr, const u16* __restrict__ wg, const u16* __restrict__ wu, const u16* __restrict__ wd) {
    u64 pc0; asm volatile("s_getpc_b64 %0" : "=s"(pc0));
    asm volatile("" :: "s"(x), "s"(wqkv), "s"(ws), "s"(wo), "s"(out), "s"(wr), "s"(wg), "s"(wu), "s"(wd));
    __shared__ __attribute__((aligned(1024))) char lds_mem[L_TOTAL];
    const u32 LB = (u32)(uintptr_t)lds_mem;
    int b = blockIdx.x, tid = threadIdx.x;
    const int wv = __builtin_amdgcn_readfirstlane(tid >> 6);
    if (wv < 2) {
        const int rb = ((b & 7) >> 1) * 8 + (b >> 5), hq = (b & 1) * 4 + ((b >> 3) & 3), rot = (rb + ROTX * hq) & 3, lane = tid & 63;
        const i32x4 rx = mk_rsrc(x, 2048 * 512 * 2), rw = mk_rsrc(wqkv, 512 * 1536 * 2);
        const u32 vx0 = (u32)(lane >> 3) * 1024 + (((lane & 7) ^ ((lane >> 4) & 3)) << 4), vx1 = vx0 ^ 64;
        u32 so = (u32)(rb * 64) * 1024 + (u32)rot * 256 + wv * 128, ld = LB + wv * GR;
#pragma unroll
        for (int j = 0; j < 8; j++) { dma16(ld, (j & 1) ? vx1 : vx0, rx, so); ld += 1024; so += 8 * 1024; }
        const u32 vw = (u32)(lane >> 3) * 3072 + (((lane & 7) ^ (((lane >> 4) & 1) << 2)) << 4);
        so = ((u32)rot * 128 + wv * 64) * 3072 + 1024 + hq * 128; ld = LB + wv * GR + 4 * SL;
#pragma unroll
        for (int j = 0; j < 8; j++) { dma16(ld, vw, rw, so); ld += 1024; so += 8 * 3072; }
    }
    asm volatile("" : "+s"(b), "+v"(tid));
    const int xcd = b & 7, bi = b >> 3;
    const int rb = (xcd >> 1) * 8 + (bi >> 2), hq = (xcd & 1) * 4 + (bi & 3);
    const int lane = tid & 63;
    const int rot = (rb + ROTX * hq) & 3;
    const int h = lane >> 5, l31 = lane & 31;
#define STAMP_OUT() do {} while (0)

    const u32 flg = LB + L_FLG;
    const i32x4 rs_ws = mk_rsrc(ws, WS_USED);
    const __amdgpu_buffer_rsrc_t bws = __builtin_amdgcn_make_buffer_rsrc(ws, 0, WS_USED, 0x00020000);
    const u32 l16 = opaque(lane * 16);
    u32 spins = 0, pt = 0;
    u32 tag;
    f32x16 acc, zero;
#pragma unroll
    for (int i = 0; i < 16; i++) zero[i] = 0.f;
    const int cw = wv & 3, rg = cw & 1, pr = cw >> 1, hd = wv < 4 ? 1 : 0;
    const int g = rb * 2 + rg, pair = hq * 2 + pr;
    const int g0 = rb * 2 - 8 + rg * 4;
    const u32 own = LB + L_OWN + cw * 4096;
    const u32 vlo = h * 256 + (lane & 15) * 16;
    u32 hso[4];
    u32 dmask[8];
    u32x4 kf[4];
    u32x4 vf[4][2];
    const u32 kbits = 0xfu << (pr * 8 + rg * 4), vbits = kbits << 16;
    auto prep = [&]() {
#pragma unroll
        for (int t = 0; t < 4; t++) { hso[t] = g0 + t >= 0 ? (u32)((g0 + t) * 32 + pair * 2 + hd) * 1024 : 0x40000000u; asm volatile("" : "+s"(hso[t])); }
        const u32 mbits = (u32)((2ull << l31) - 1ull) >> (4 * h);
#pragma unroll
        for (int i = 0; i < 8; i++) { const int k = ((2 * i) & 3) + 8 * (i >> 1);
            dmask[i] = opaque(((u32)__builtin_amdgcn_sbfe(mbits, k, 1) & 0xffffu) | ((u32)__builtin_amdgcn_sbfe(mbits, k + 1, 1) & 0xffff0000u)); }
    };
    auto ld_k = [&]() {
#pragma unroll
        for (int t = 0; t < 4; t++) kf[t] = __builtin_amdgcn_raw_buffer_load_b128(bws, l16, WS_K + hso[t], 16); };
    auto ld_v = [&]() {
        const u32 vab = (lane & 16) ? WS_ONES + b * 1024 : WS_V + vlo, vam = (lane & 16) ? 0x40000000u : 0xffffffffu;
#pragma unroll
        for (int tt = 0; tt < 4; tt++) { const int t = (tt + 2) & 3; const u32 va = vab + (hso[t] & vam);
            vf[t][0] = __builtin_amdgcn_raw_buffer_load_b128(bws, va, 0, 16); vf[t][1] = __builtin_amdgcn_raw_buffer_load_b128(bws, va + 512, 0, 16); } };
    auto st_flag = [&](u32 off) { __builtin_amdgcn_raw_buffer_store_b32(tag, bws, 0, off, 16); };

    auto fill_o = [&](int sl, int j0, int j1) {
        const i32x4 ro = mk_rsrc(wo, 512 * 512 * 2);
        const u32 vo_w = (u32)(lane >> 4) * 1024 + (((lane & 15) ^ (((lane >> 4) & 3) << 2)) << 4);
        u32 so = (u32)(sl ^ (rb & 7)) * 32768 + (hq >> 1) * 256 + j0 * 4096, ld = LB + sl * SL + j0 * 1024;
#pragma unroll
        for (int j = j0; j < j1; j++) { dma16(ld, vo_w, ro, so); ld += 1024; so += 4096; }
    };
    if (__builtin_expect(wv < 4, 1)) {
        if (__builtin_expect(wv < 2, 1)) {
            __builtin_amdgcn_s_setprio(3);
            const int lw = wv;
            const u32 GB = LB + lw * GR;
            const i32x4 rx = mk_rsrc(x, 2048 * 512 * 2), rw = mk_rsrc(wqkv, 512 * 1536 * 2);
            const u32 vx0 = (u32)(lane >> 3) * 1024 + (((lane & 7) ^ ((lane >> 4) & 3)) << 4), vx1 = vx0 ^ 64;
            auto fill_x = [&](int bk) {
                u32 so = (u32)(rb * 64) * 1024 + (u32)((bk + rot) & 3) * 256 + lw * 128, ld = GB + bk * SL;
#pragma unroll
                for (int j = 0; j < 8; j++) { dma16(ld, (j & 1) ? vx1 : vx0, rx, so); ld += 1024; so += 8 * 1024; }
            };
            const u32 vw = (u32)(lane >> 3) * 3072 + (((lane & 7) ^ (((lane >> 4) & 1) << 2)) << 4);
            auto fill = [&](int f) {
                const int seg = (f >> 2) == 0 ? 1 : (f >> 2) == 1 ? 2 : 0;
                u32 so = ((u32)((f + rot) & 3) * 128 + lw * 64) * 3072 + seg * 1024 + hq * 128;
                u32 ld = GB + fill_slot(f) * SL;
#pragma unroll
                for (int j = 0; j < 8; j++) { dma16(ld, vw, rw, so); ld += 1024; so += 8 * 3072; }
            };
            fill_x(1); fill(1);
            WAITV(16); STAMP(1); RBAR();
            fill_x(2); fill(2);
            WAITV(16); RBAR();
            fill_x(3); fill(3);
            WAITV(16); RBAR();
            fill(4); fill(5);
            WAITV(16); STAMP(2); RBAR();
            fill(6);
            WAITV(16); RBAR();
            fill(7); prep();
            WAITV(16); RBAR();
            fill(8);
            WAITV(16); RBAR();
            fill(9);
            WAITV(16); STAMP(3); RBAR();
            fill(10);
            {
                const i32x4 rf = mk_rsrc(lw ? wu : wg, b < 224 ? 512 * 1792 * 2 : 0);
                const u32 vf_ = (u32)(lane >> 3) * 3584 + (((lane & 7) ^ (((lane >> 4) & 1) << 2)) << 4);
                u32 so = (u32)(b & 7) * 64 * 3584 + (u32)(b >> 3) * 128, ld = GB;
#pragma unroll
                for (int j = 0; j < 8; j++) { dma16(ld, vf_, rf, so); ld += 1024; so += 8 * 3584; }
            }
            WAITV(24); RBAR();
            fill(11);
            WAITV(24); RBAR();
            fill_o(lw * 8 + 4, 0, 8);
            WAITV(8); STAMP(4); lds_sig(flg + F_LAND * 4);
            fill_o(lw * 8 + 1, 0, 8); WAITV(8); fill_o(lw * 8 + 5, 0, 8);
            __builtin_amdgcn_s_setprio(0);
        } else {
            const u32x4 o4 = {ONES, ONES, ONES, ONES};
            *(LAS u32*)(LB + L_FLG + (tid - 128) * 4) = 0u;
            if (__builtin_expect(wv == 2, 1)) {
                const u32 ep_v = __hip_atomic_load((const GAS u32*)(ws + WS_EPOCH), RLX_AGENT);
                lds_w128(LB + L_ONES + l16, o4); st_sc1(bws, WS_ONES + b * 1024 + l16, o4);
                BAR();
                *(volatile LAS u32*)(flg + F_TAG * 4) = ep_v + 1u;
                BAR();
                prep();
#pragma unroll
                for (int i = 2; i < 9; i++) RBAR();
                WAITV(0); RBAR();
            } else {
                const int rrow = rb * 64 + hq * 8 + (lane >> 3);
                u32x4 xr[8], wr_[8];
#pragma unroll
                for (int i = 0; i < 8; i++) { xr[i] = *(const GAS u32x4*)(x + (size_t)rrow * 512 + i * 64 + (lane & 7) * 8); wr_[i] = *(const GAS u32x4*)(wr + i * 64 + (lane & 7) * 8); }
                BAR();
                float a0 = 0, a1 = 0, a2 = 0, a3 = 0;
#pragma unroll
                for (int i = 0; i < 8; i++) {
#pragma unroll
                    for (int e = 0; e < 4; e++) { a0 = dot2(xr[i][e], ONES, a0); a1 = dot2(xr[i][e], xr[i][e], a1); a2 = dot2(xr[i][e], wr_[i][e], a2); a3 = dot2(wr_[i][e], ONES, a3); }
                    if (i == 3) { asm volatile("" : "+v"(a0), "+v"(a1), "+v"(a2), "+v"(a3)); RBAR(); }
                }
                asm volatile("" : "+v"(a0), "+v"(a1), "+v"(a2), "+v"(a3));
                RBAR();
                tag = __builtin_amdgcn_readfirstlane(*(volatile LAS u32*)(flg + F_TAG * 4));
                {
                    const float mean = red8(a0) * (1.0f / 512.0f), var = fmaxf(red8(a1) * (1.0f / 512.0f) - mean * mean, 0.f);
                    const float m = rbf(mean), sd = rbf(__builtin_amdgcn_sqrtf(rbf(rbf(var) + 1e-5f)));
                    const u32 rbits = pk_bf16((red8(a2) - m * red8(a3)) * __builtin_amdgcn_rcpf(sd), 0.f) & 0xffffu;
                    const u32 key = (((rbits & 0x8000u) ? (~rbits & 0xffffu) : (rbits | 0x8000u)) << 11) | (u32)(2047 - rrow);
                    if ((lane & 7) < 2) __builtin_amdgcn_raw_buffer_store_b64((u32x2){(lane & 1) ? pk_bf16(m, sd) : key, tag}, bws, ((lane & 1) ? WS_S : WS_R) + rrow * 8, 0, 16);
                }
                RBAR(); prep(); RBAR(); RBAR();
                u32x4 kq[16];
                auto gather = [&]() {
#pragma unroll
                    for (int i = 0; i < 16; i++) kq[i] = __builtin_amdgcn_raw_buffer_load_b128(bws, l16 + i * 1024, WS_R, 16);
                    asm volatile("" ::: "memory"); };
                auto stale = [&]() { u32 d = 0;
#pragma unroll
                    for (int i = 0; i < 16; i++) d |= (kq[i].y ^ tag) | (kq[i].w ^ tag);
                    return !__all(d == 0u); };
                u32 m1 = 0, m2 = 0, m3 = 0, base = 0;
                auto top3 = [&](int i0, int i1, bool rel) {
#pragma unroll
                    for (int i = i0; i < i1; i++) { u32 k = kq[i >> 1][(i & 1) * 2]; if (rel) k -= base;
                        asm("v_med3_u32 %0, %1, %0, %2" : "+v"(m3) : "v"(m2), "v"(k)); asm("v_med3_u32 %0, %1, %0, %2" : "+v"(m2) : "v"(m1), "v"(k)); m1 = m1 > k ? m1 : k; }
                    asm volatile("" : "+v"(m1), "+v"(m2), "+v"(m3)); };
                gather();
                RBAR(); RBAR();
                const bool bad = stale();
                if (__builtin_expect(bad, 0)) gather(); else top3(0, 10, false);
                RBAR();
                if (!bad) top3(10, 32, false);
                RBAR();
                u32 left = 3;
                if (__builtin_expect(bad, 0)) { while (stale() && ++spins <= SPIN_CAP) gather(); left = 0; }
                STAMP(7);
                u32 sel = 0, lim = 1u << 27;
#pragma unroll 1
                for (int r = 0; r < 8; r++) {
                    if (__builtin_expect(__any(left == 0u), 0)) { m1 = 0; m2 = 0; m3 = 0; base = lim; left = 3; top3(0, 32, true); }
                    const u32 t = wave_umax(m1);
                    lim = t + base;
                    if (m1 == t) { m1 = m2; m2 = m3; left--; }
                    if ((lane & 7) == r) sel = lim;
                }
                if (lane < 8) *(LAS u32*)(LB + L_SEL + lane * 4) = sel;
                STAMP(8);
                if (b < 224) {
                    const int srow = 2047 - (int)(sel & 2047u);
                    const u16* xp = x + (size_t)srow * 512 + (b & 7) * 64 + (lane >> 4) * 8;
                    u32x2 st = __builtin_amdgcn_raw_buffer_load_b64(bws, WS_S + srow * 8, 0, 16);
                    u32x4 xs0 = *(const GAS u32x4*)xp, xs1 = *(const GAS u32x4*)(xp + 32);
                    asm volatile("" : "+v"(xs0), "+v"(xs1) :: "memory");
                    while (!__all(st.y == tag) && ++spins <= SPIN_CAP) { st = __builtin_amdgcn_raw_buffer_load_b64(bws, WS_S + srow * 8, 0, 16); asm volatile("" ::: "memory"); }
                    const float m = bf_lo(st.x), rs = __builtin_amdgcn_rcpf(bf_hi(st.x));
#pragma unroll
                    for (int e = 0; e < 4; e++) { xs0[e] = norm2(xs0[e], m, rs); xs1[e] = norm2(xs1[e], m, rs); }
                    lds_cnt(flg + F_LAND * 4, 2);
                    const u32 wa = LB + (u32)(8 * (lane >> 4) + ((lane & 15) >> 2)) * 128 + ((lane & 3) << 3), sw = ((lane >> 3) & 1) << 6;
                    const u32 go = WS_GU + (u32)b * 4096 + ((lane >> 4) * 32 + (lane & 8) * 2 + (lane & 7)) * 8;
#pragma unroll
                    for (int ct = 0; ct < 4; ct++) {
                        f32x4 d[2];
#pragma unroll
                        for (int mt = 0; mt < 2; mt++) {
                            const u32 a = wa + mt * GR + ((((ct >> 1) << 6) ^ sw) + (ct & 1) * 32);
                            d[mt] = mfma16(cat(tr_read(a), tr_read(a + 512)), xs0, bc4(0.f));
                            d[mt] = mfma16(cat(tr_read(a + 4096), tr_read(a + 4096 + 512)), xs1, d[mt]);
                        }
#pragma unroll
                        for (int e = 0; e < 2; e++) {
                            const float gq = (lane & 8) ? d[0][2 + e] : d[0][e], uq = (lane & 8) ? d[1][2 + e] : d[1][e];
                            __builtin_amdgcn_raw_buffer_store_b64((u32x2){__float_as_uint(gq), __float_as_uint(uq)}, bws, go + (ct * 16 + e) * 64, 0, 16);
                        }
                    }
                } else lds_cnt(flg + F_LAND * 4, 2);
                STAMP(9);
            }
        }
        tag = __builtin_amdgcn_readfirstlane(*(volatile LAS u32*)(flg + F_TAG * 4));
        if (wv == 2) {
            const int gg = rb * 2 - 8 + (lane & 7);
            const GAS u32* fp = (const GAS u32*)(ws + ((lane & 16) ? WS_FLAGV : WS_FLAGK) + ((gg < 0 ? 0 : gg) * 16 + hq * 2 + ((lane >> 3) & 1)) * FS);
            for (;;) {
                const u32 ok = (u32)__ballot(gg < 0 || __hip_atomic_load(fp, RLX_AGENT) == tag);
                const bool done = ok == 0xffffffffu || ++spins > SPIN_CAP;
                if (lane == 0) *(volatile LAS u32*)(flg + F_HALO * 4) = done ? 0xffffffffu : ok;
                if (done) break;
            }
        }
        lds_bits(flg + F_HALO * 4, kbits);
        STAMP(5);
        ld_k();
        lds_bits(flg + F_HALO * 4, vbits);
        ld_v();
        STAMP(6);
    } else {
    __builtin_amdgcn_s_setprio(2);
    u32 xfa[2][4], wfa[2][4];
    {
        const int row = rg * 32 + l31;
        const u32 w0 = LB + (u32)(8 * h + ((lane & 15) >> 2)) * 128 + ((pr ^ ((lane >> 3) & 1)) << 6) + (((lane >> 4) & 1) << 5) + ((lane & 3) << 3);
#pragma unroll
        for (int s = 0; s < 4; s++) {
            xfa[0][s] = opaque(LB + (row >> 3) * 1024 + (row & 7) * 128 + (((2 * (s ^ cw) + h) ^ ((row >> 1) & 7)) << 4)); xfa[1][s] = opaque(xfa[0][s] + GR);
            wfa[0][s] = opaque(w0 + (s ^ cw) * 2048); wfa[1][s] = opaque(w0 + (s ^ cw) * 2048 + GR);
        }
    }
    const u32 csw = opaque(LB + L_CS + (rg * 64 + pr * 32 + ((lane & 1) << 4) + (lane >> 4) * 4) * 4), csr = opaque(LB + L_CS + (h * 64 + pr * 32 + l31) * 4);
    prep();
    const u32 selv = opaque((((lane >> 4) ^ lane) & 1) ? 0u : ONES);
    const u32x4 sel = {selv, selv, selv, selv};
    u32 blk = (u32)(g * 32 + pair * 2) * 1024, fo = (u32)(g * 16 + pair) * FS;
    asm volatile("" : "+s"(blk), "+s"(fo));
    const u32 vst = opaque(((lane >> 4) & 1) * 1024 + vlo);
    const u32 sta = opaque(LB + L_STP + (rg * 32 + l31) * 16);
    u32x4 xf[8][4], wf[2][4];
    f32x16 acc2;
    f32x4 cs;
    float sx0 = 0, sx1 = 0, sq0 = 0, sq1 = 0;
    auto rw_ = [&](int n, int s) {
        const u32 a = wfa[n & 1][s] + fill_slot(n >> 1) * SL;
        const u32x2 lo = tr_read(a), hi = tr_read(a + 512); wf[n & 1][s] = (u32x4){lo.x, lo.y, hi.x, hi.y};
        if (n < 8) xf[n][s] = lds_r128(xfa[n & 1][s] + (n >> 1) * SL);
    };
    auto mm_ = [&](int n, int s, f32x16& a) {
        const f32x16 c = ((n & 7) == 0 && s == 0) ? zero : a;
        a = (n >> 3) != 1 ? mfma32(wf[n & 1][s], xf[n & 7][s], c) : mfma32(xf[n & 7][s], wf[n & 1][s], c);
        PIN4(a);
    };
    auto cs_ = [&](int n, int s) { cs = mfma16(wf[n & 1][s], sel, ((n & 7) == 0 && s == 0) ? bc4(0.f) : cs); PIN4(cs); };
    auto st_ = [&](int i) {
#pragma unroll
        for (int s = 0; s < 2; s++) { const u32x4 v = xf[i][s];
            sx0 = dot2(v.x, ONES, sx0); sq0 = dot2(v.x, v.x, sq0); sx1 = dot2(v.y, ONES, sx1); sq1 = dot2(v.y, v.y, sq1);
            sx0 = dot2(v.z, ONES, sx0); sq0 = dot2(v.z, v.z, sq0); sx1 = dot2(v.w, ONES, sx1); sq1 = dot2(v.w, v.w, sq1); }
        asm volatile("" : "+v"(sx0), "+v"(sx1), "+v"(sq0), "+v"(sq1));
    };
#define RW(n, s) do { if ((n) < 24) rw_(n, s); } while (0)
#define STEP(n, A, X0, X1, X2, X3) do { STAMPS_STEP(n); \
        mm_(n, 0, A); SB(); RW(n + 1, 0); X0; SB(); cs_(n, 0); SB(); \
        mm_(n, 1, A); SB(); RW(n + 1, 1); X1; SB(); \
        mm_(n, 2, A); SB(); RW(n + 1, 2); X2; SB(); cs_(n, 2); SB(); \
        mm_(n, 3, A); SB(); RW(n + 1, 3); X3; SB(); if ((n) < 8) st_(n); if (!((n) & 1) && (n) <= 16) { if ((n) == 8 || (n) == 16) asm volatile("s_waitcnt lgkmcnt(8)" ::: "memory"); RBAR(); } if ((n) == 18) lds_cnt(flg + F_LAND * 4, 2); SB(); } while (0)
#define NOP_ do {} while (0)
#define STEP0(n, A) STEP(n, A, NOP_, NOP_, NOP_, NOP_)
    STAMP(1);
    RBAR();
    STAMP(2);
#pragma unroll
    for (int s = 0; s < 4; s++) rw_(0, s);
    SB();
    STEP0(0, acc); STEP0(1, acc); STEP0(2, acc); STEP0(3, acc); STEP0(4, acc); STEP0(5, acc); STEP0(6, acc); STEP0(7, acc);
    STAMP(5);
    {
        auto sw = __builtin_amdgcn_permlane32_swap(__float_as_uint(sx0 + sx1), __float_as_uint(sq0 + sq1), false, false);
        lds_wf(sta + h * 8 + pr * 4, __uint_as_float(sw[0]) + __uint_as_float(sw[1]));
        lds_wf4(csw, cs);
    }
    SB();
    float rs_t;
    u32x4 mfr;
    u32x4 kA, kB;
    {
        f32x4 sp; float mean, var, sd, c; u32 mp; u32x4 cf; u32x2 k0, k1;
        STEP0(8, acc2);
        sp = lds_rf4(sta); c = lds_rf(csr); SB();
        STEP(9, acc2, (mean = np(np(sp.x + sp.y) * (-1.0f / 512.0f)), mp = pk_bf16(mean, mean), mfr = (u32x4){mp, mp & 0xffffu, 0u, 0u}), (cf = split3(c)),
             (acc = mfma32(cf, mfr, acc), var = np(np(sp.z + sp.w) * (1.0f / 512.0f)), var = rbf(fmaxf(var - mean * mean, 0.f))),
             (sd = rbf(__builtin_amdgcn_sqrtf(rbf(var + 1e-5f))), rs_t = __builtin_amdgcn_rcpf(sd)));
        STEP(10, acc2, (k0 = scl4(Q4(acc, 0), bc4(rs_t))), (k1 = scl4(Q4(acc, 1), bc4(rs_t)), kA = cat(k0, k1), st_sc1(bws, WS_K + blk + l16, kA)), (k0 = scl4(Q4(acc, 2), bc4(rs_t))),
             (k1 = scl4(Q4(acc, 3), bc4(rs_t)), kB = cat(k0, k1), st_sc1(bws, WS_K + 1024 + blk + l16, kB)));
        STAMP(6);
        STEP(11, acc2, (lds_wf(LB + L_ST + (rg * 32 + l31) * 4 + h * 256, h ? -bf_lo(mp) : rs_t)), lds_w128(own + l16, kA), NOP_, lds_w128(own + 1024 + l16, kB));
    }
    u32 tg_;
    STEP(12, acc2, (tg_ = *(volatile LAS u32*)(flg + F_TAG * 4)), NOP_, NOP_, (tag = __builtin_amdgcn_readfirstlane(tg_))); STEP0(13, acc2);
    STEP(14, acc2, NOP_, NOP_, NOP_, (WAITV(0), st_flag(WS_FLAGK + fo)));
    STAMP(7);
    STEP0(15, acc2);
    lds_wf4(csw + 512, cs);
    SB();
    {
        const u32 sa = LB + L_ST + (rg * 32 + 4 * h) * 4;
        f32x4 r0, r1, r2, r3; float c; u32x4 cf, vS; u32x2 v0, v1;
        STEP0(16, acc);
        c = lds_rf(csr + 512); SB();
        STEP(17, acc, (cf = split3(c)), (acc2 = mfma32(mfr, cf, acc2), r0 = lds_rf4(sa), r1 = lds_rf4(sa + 32)), (r2 = lds_rf4(sa + 64), r3 = lds_rf4(sa + 96)), (v0 = scl4(Q4(acc2, 0), r0)));
        STEP(18, acc, (v1 = scl4(Q4(acc2, 1), r1)), (vS = cat(v0, v1), st_sc1(bws, WS_V + blk + vst, vS), lds_w128(own + 2048 + vst, vS)), (v0 = scl4(Q4(acc2, 2), r2)),
             (v1 = scl4(Q4(acc2, 3), r3), vS = cat(v0, v1), st_sc1(bws, WS_V + 512 + blk + vst, vS), lds_w128(own + 2048 + 512 + vst, vS)));
    }
    STAMP(8);
    STEP0(19, acc); STEP0(20, acc); STEP0(21, acc);
    STEP(22, acc, NOP_, NOP_, NOP_, (WAITV(0), st_flag(WS_FLAGV + fo)));
    STAMP(9);
    {
        cs_(23, 0); cs_(23, 2); SB();
        mm_(23, 0, acc); SB(); lds_wf4(csw + 1024, cs); lds_sig(flg + F_CS * 4); SB();
        mm_(23, 1, acc); SB(); lds_bits(flg + F_HALO * 4, kbits); ld_k(); SB();
        STAMP(10);
        mm_(23, 2, acc); SB(); lds_cnt(flg + F_CS * 4, 4); const float c = lds_rf(csr + 1024); SB();
        mm_(23, 3, acc); SB();
        acc = mfma32(split3(c), mfr, acc);
        const f32x4 sF = bc4(rs_t * (0.25f * 1.4426950408889634f));
        const u32x4 qA = cat(scl4(Q4(acc, 0), sF), scl4(Q4(acc, 1), sF)), qB = cat(scl4(Q4(acc, 2), sF), scl4(Q4(acc, 3), sF));
        lds_w128(LB + L_Q + cw * 2048 + l16, qA); lds_w128(LB + L_Q + cw * 2048 + 1024 + l16, qB);
        float m = dot2(qA.x, kA.x, 0.f), m2 = dot2(qB.x, kB.x, 0.f);
        m = dot2(qA.y, kA.y, m); m2 = dot2(qB.y, kB.y, m2); m = dot2(qA.z, kA.z, m); m2 = dot2(qB.z, kB.z, m2); m = dot2(qA.w, kA.w, m); m2 = dot2(qB.w, kB.w, m2);
        auto sw = __builtin_amdgcn_permlane32_swap(__float_as_uint(m), __float_as_uint(m2), false, false);
        lds_wf(LB + L_M + cw * 256 + lane * 4, -(__uint_as_float(sw[0]) + __uint_as_float(sw[1])));
    }
    lds_sig(flg + F_Q * 4);
    STAMP(11);
    }
    __builtin_amdgcn_s_setprio(1);
    lds_cnt(flg + F_Q * 4, 4);

    STAMP(12);
    const u32x4 qF = lds_r128(LB + L_Q + cw * 2048 + hd * 1024 + l16), kO = lds_r128(own + hd * 1024 + l16);
    const float mF = lds_rf(LB + L_M + cw * 256 + hd * 128 + l31 * 4);
    const u32 ownv = (lane & 16) ? LB + L_ONES : own + 2048 + hd * 1024 + vlo;
    u32x4 vX0 = lds_r128(ownv), vX1 = lds_r128(ownv + 512), kX = kO;
    const u32x4 qS = lds_r128(LB + L_Q + (cw ^ 1) * 2048 + hd * 1024 + l16);
    const float mS = lds_rf(LB + L_M + (cw ^ 1) * 256 + hd * 128 + l31 * 4);
    f32x16 cF, cS, oF, oS, sa, sb;
#pragma unroll
    for (int i = 0; i < 16; i++) cF[i] = mF;
    sa = mfp(kO, qF, cF); SB();
#pragma unroll
    for (int i = 0; i < 16; i++) cS[i] = mS;
    PIN4(cS); SB();
    u32x4 pa[2], pb[2], pc[2], pd[2];
#define EX2(S, i) pkp(__builtin_amdgcn_exp2f(S[2 * (i)]), __builtin_amdgcn_exp2f(S[2 * (i) + 1]))
#define TILE(S, MASK, P, M0, M1, M2, M3) do { \
        P[0][0] = EX2(S, 0); P[0][1] = EX2(S, 1); SB(); M0; SB(); P[0][2] = EX2(S, 2); P[0][3] = EX2(S, 3); \
        if (MASK) { _Pragma("unroll") for (int i = 0; i < 4; i++) P[0][i] = opaque(P[0][i] & dmask[i]); } SB(); M1; SB(); \
        P[1][0] = EX2(S, 4); P[1][1] = EX2(S, 5); SB(); M2; SB(); P[1][2] = EX2(S, 6); P[1][3] = EX2(S, 7); \
        if (MASK) { _Pragma("unroll") for (int i = 0; i < 4; i++) P[1][i] = opaque(P[1][i] & dmask[4 + i]); } SB(); M3; SB(); } while (0)
    TILE(sa, true, pa, NOP_, (oF = mfp(vX0, pa[0], zero)), (sb = mfp(kf[2], qS, cS)), (oF = mfp(vX1, pa[1], oF)));
    STAMP(13);
    asm volatile("" : "+v"(kf[0]), "+v"(kf[1]), "+v"(kf[3]));
    if (!hd) { lds_bits(flg + F_HALO * 4, vbits); ld_v(); }
    else if (wv == 3) { WAITV(0); st_flag(WS_FGU + b * FS); }
    const int erow = (hq & 1) * 32 + (tid >> 4);
    const int orow = rb * 64 + erow, ocol = (hq >> 1) * 128 + (tid & 15) * 8;
    u32x4 xe = *(const GAS u32x4*)(x + (size_t)orow * 512 + ocol);
    STAMP(14);
    {
        const u32 om = rg ? 0xffffffffu : 0u;
#pragma unroll
        for (int i = 0; i < 8; i++) dmask[i] = opaque(~dmask[i] | om);
        if (rg) kX = kf[0];
        PIN4(kX);
    }
    SB();
    TILE(sb, false, pa, (sa = mfp(kf[3], qS, cS)), NOP_, NOP_, (sb = mfp(kf[1], qS, cS)));
    TILE(sa, false, pb, NOP_, NOP_, NOP_, (sa = mfp(kX, qS, cS)));
    STAMP(15);
    TILE(sb, true, pc, (oS = mfp(vf[2][0], pa[0], zero)), (oS = mfp(vf[2][1], pa[1], oS)), (oS = mfp(vf[3][0], pb[0], oS)), (sb = mfp(kf[0], qF, cF)));
    if (rg) { vX0 = vf[0][0]; vX1 = vf[0][1]; }
    PIN4(vX0); PIN4(vX1);
    asm volatile("" : "+v"(vf[0][0]), "+v"(vf[0][1]), "+v"(vf[1][0]), "+v"(vf[1][1]), "+v"(xe));
    STAMP(16);
    const int osl = (wv >> 1) * 8 + (wv & 1) * 4 + 2;
#define WO(j) do { if (hd) fill_o(osl + ((j) >> 1), ((j) & 1) * 4, ((j) & 1) * 4 + 4); SB(); } while (0)
    WO(0);
    if (wv == 3) { fill_o(0, 0, 8); fill_o(8, 0, 8); }
    u32x4 wdr[8];
    if (wv == 0 && b < 224) {
#pragma unroll
        for (int i = 0; i < 8; i++) wdr[i] = *(const GAS u32x4*)(wd + (size_t)((b >> 3) * 64 + i * 8 + (lane >> 3)) * 512 + (b & 7) * 64 + (((lane & 7) ^ (((lane >> 4) & 1) << 2)) << 3));
    }
    SB();
    TILE(sa, false, pd, (oS = mfp(vf[3][1], pb[1], oS)), (oS = mfp(vf[1][0], pc[0], oS)), (oS = mfp(vf[1][1], pc[1], oS)), (sa = mfp(kf[1], qF, cF)));
    WO(1);
    TILE(sb, true, pa, (oS = mfp(vX0, pd[0], oS)), (oS = mfp(vX1, pd[1], oS)), NOP_, (sb = mfp(kf[2], qF, cF)));
    WO(2);
    const u32 oxw = LB + L_OWN + cw * 4096 + hd * 1024;
    TILE(sa, false, pb, (oF = mfp(vf[0][0], pa[0], oF)), (lds_wf4(oxw + l16, Q4(oS, 0)), lds_wf4(oxw + 2048 + l16, Q4(oS, 1)), lds_wf(LB + L_L + wv * 256 + lane * 4, oS[8]), lds_sig(flg + (F_O + wv) * 4)),
         (oF = mfp(vf[0][1], pa[1], oF)), (sa = mfp(kf[3], qF, cF)));
    STAMP(17);
    WO(3);
    TILE(sb, false, pc, (oF = mfp(vf[1][0], pb[0], oF)), NOP_, (oF = mfp(vf[1][1], pb[1], oF)), (oF = mfp(vf[2][0], pc[0], oF)));
    TILE(sa, false, pd, (oF = mfp(vf[2][1], pc[1], oF)), NOP_, (oF = mfp(vf[3][0], pd[0], oF)), (oF = mfp(vf[3][1], pd[1], oF)));
    STAMP(19);
    const GAS u32* fg = (const GAS u32*)(ws + WS_FGU + (b < 224 ? (b & ~7) + (lane & 7) : b) * FS);
    u32 gv_ = __hip_atomic_load(fg, RLX_AGENT);
    lds_cnt(flg + (F_O + (wv ^ 1)) * 4, 1);
    STAMP(20);
    {
        const u32 e = LB + L_OWN + (cw ^ 1) * 4096 + hd * 1024;
        const f32x4 t0 = lds_rf4(e + l16), t1 = lds_rf4(e + 2048 + l16); const float tl = lds_rf(LB + L_L + (wv ^ 1) * 256 + lane * 4);
        float l = oF[8] + tl;
        f32x4 o0 = add4(Q4(oF, 0), t0), o1 = add4(Q4(oF, 1), t1);
        const float amax = fmaxf(fmaxf(fmaxf(fabsf(o0.x), fabsf(o0.y)), fmaxf(fabsf(o0.z), fabsf(o0.w))), fmaxf(fmaxf(fabsf(o1.x), fabsf(o1.y)), fmaxf(fabsf(o1.z), fabsf(o1.w))));
        if (__builtin_expect(FORCE_FB || !__all(l >= 0x1p-60f && l <= 0x1p60f && amax <= 0x1p120f && o0.x + o0.y + o0.z + o0.w + o1.x + o1.y + o1.z + o1.w == o0.x + o0.y + o0.z + o0.w + o1.x + o1.y + o1.z + o1.w), 0)) {
            const int j0 = g >= 8 ? g - 8 : 0, head = pair * 2 + hd;
            { int gg = j0 + (lane & 15); gg = gg > g ? g : gg;
              const GAS u32* fp = (const GAS u32*)(ws + ((lane & 16) ? WS_FLAGV : WS_FLAGK) + (gg * 16 + pair) * FS);
              while (!__all(__hip_atomic_load(fp, RLX_AGENT) == tag) && ++spins <= SPIN_CAP) {}
              __builtin_amdgcn_fence(__ATOMIC_ACQUIRE, "wavefront"); }
            float mx = -3.0e38f;
            f32x16 o = zero;
#pragma unroll 1
            for (int pass = 0; pass < 2; pass++) {
#pragma unroll 1
                for (int j = j0; j <= g; j++) {
                    const u32 bo = (u32)(j * 32 + head) * 1024;
                    const f32x16 sc = mfma32(__builtin_amdgcn_raw_buffer_load_b128(bws, l16, WS_K + bo, 16), qF, zero);
                    const int dq = (g - j) * 32 + l31 - 4 * h;
                    float p[16];
#pragma unroll
                    for (int i = 0; i < 16; i++) { const int d = dq - (i & 3) - 8 * (i >> 2); const bool vis = d >= 0 && d < 256;
                        if (vis) mx = pass ? mx : fmaxf(mx, sc[i]); p[i] = vis ? __builtin_amdgcn_exp2f(sc[i] - mx) : 0.f; }
                    if (pass) {
                        const u32 va = (lane & 16) ? WS_ONES + b * 1024 : WS_V + vlo + bo;
                        o = mfma32(__builtin_amdgcn_raw_buffer_load_b128(bws, va, 0, 16), (u32x4){pk_bf16(p[0], p[1]), pk_bf16(p[2], p[3]), pk_bf16(p[4], p[5]), pk_bf16(p[6], p[7])}, o);
                        o = mfma32(__builtin_amdgcn_raw_buffer_load_b128(bws, va + 512, 0, 16), (u32x4){pk_bf16(p[8], p[9]), pk_bf16(p[10], p[11]), pk_bf16(p[12], p[13]), pk_bf16(p[14], p[15])}, o);
                    }
                }
                auto sw = __builtin_amdgcn_permlane32_swap(__float_as_uint(mx), __float_as_uint(mx), false, false);
                mx = fmaxf(__uint_as_float(sw[0]), __uint_as_float(sw[1]));
            }
            l = o[8]; o0 = Q4(o, 0); o1 = Q4(o, 1);
        }
        const float inv = __builtin_amdgcn_rcpf(l);
        st_sc1(bws, WS_O + (u32)(g * 32 + pair * 2 + hd) * 1024 + l16, cat(scl4(o0, bc4(inv)), scl4(o1, bc4(inv))));
    }
    STAMP(21);
    __builtin_amdgcn_s_setprio(0);
    SB();
    u32x2 gu[8];
    while (!__all(gv_ == tag) && ++spins <= SPIN_CAP) gv_ = __hip_atomic_load(fg, RLX_AGENT);
    __builtin_amdgcn_fence(__ATOMIC_ACQUIRE, "wavefront");
#pragma unroll
    for (int i = 0; i < 8; i++) gu[i] = __builtin_amdgcn_raw_buffer_load_b64(bws, lane * 8 + wv * 512, WS_GU + (u32)((b & ~7) + i) * 4096, 16);
    SB();
    u32 gp[4], xnp[4];
    {
        const float e_m = lds_rf(LB + L_ST + 256 + erow * 4), e_rs = lds_rf(LB + L_ST + erow * 4);
#pragma unroll
        for (int e = 0; e < 4; e++) { xnp[e] = norm2(xe[e], e_m, e_rs); gp[e] = opaque(pk_bf16(silu_(bf_lo(xnp[e])), silu_(bf_hi(xnp[e])))); }
    }
    SB();
    WAITV(8); st_flag(WS_FLAGO + (u32)(g * 32 + pair * 2 + hd) * FS);
    STAMP(22);
    SB();
    {
        float ga = 0, ua = 0;
#pragma unroll
        for (int i = 0; i < 8; i++) { ga += __uint_as_float(gu[i].x); ua += __uint_as_float(gu[i].y); }
        const float hv = rbf(silu_(rbf(ga))) * rbf(ua);
        *(LAS u16*)(LB + L_H + (lane & 7) * 128 + (wv * 8 + (lane >> 3)) * 2) = (u16)pk_bf16(hv, hv);
    }
    STAMP(24);
    BAR();
    STAMP(23);
    const int og = rb * 2 + (hq & 1), kh = wv >> 2;
    if (wv == 0 && b < 224) {
#pragma unroll
        for (int i = 0; i < 8; i++) lds_w128(LB + L_Q + i * 1024 + l16, wdr[i]);
        const u32x4 h0 = lds_r128(LB + L_H + (lane & 7) * 128 + (lane >> 4) * 16), h1 = lds_r128(LB + L_H + (lane & 7) * 128 + 64 + (lane >> 4) * 16);
        const u32 wa = LB + L_Q + (u32)(8 * (lane >> 4) + ((lane & 15) >> 2)) * 128 + ((lane & 3) << 3), sw = ((lane >> 3) & 1) << 6;
        const u32 yo = WS_Y + (u32)(((lane & 7) * 8 + (b & 7)) * 28 + (b >> 3)) * 512 + (lane >> 4) * 32;
#pragma unroll
        for (int ct = 0; ct < 4; ct++) {
            const u32 a = wa + ((((ct >> 1) << 6) ^ sw) + (ct & 1) * 32);
            f32x4 d = mfma16(cat(tr_read(a), tr_read(a + 512)), h0, bc4(0.f));
            d = mfma16(cat(tr_read(a + 4096), tr_read(a + 4096 + 512)), h1, d);
            const u32x4 v = (lane & 8) ? (u32x4){__float_as_uint(d.z), tag, __float_as_uint(d.w), tag} : (u32x4){__float_as_uint(d.x), tag, __float_as_uint(d.y), tag};
            st_sc1(bws, yo + ct * 128 + (lane & 8) * 2, v);
        }
    }
    const GAS u32* fq = (const GAS u32*)(ws + WS_FLAGO + (og * 32 + wv * 4 + (lane & 3)) * FS);
    u32 pv = __hip_atomic_load(fq, RLX_AGENT);
    u32x4 wof[16];
    {
        const int q = (lane & 15) >> 2;
        const u32 a = LB + kh * GR + (u32)(4 * h + q) * 256 + ((cw ^ q) << 6) + (((lane >> 4) & 1) << 5) + ((lane & 3) << 3);
#pragma unroll
        for (int s = 0; s < 16; s++) { const u32x2 lo = tr_read(a + s * 4096), hi = tr_read(a + s * 4096 + 2048); wof[s] = (u32x4){lo.x, lo.y, hi.x, hi.y}; }
        SB();
#pragma unroll
        for (int s = 0; s < 16; s++) PIN4(wof[s]);
    }
    const u32 selk = *(LAS u32*)(LB + L_SEL + (lane & 7) * 4);
    const u32 mine = (u32)__ballot(((2047u - (selk & 2047u)) >> 5) == (u32)og) & 0xffu;
    STAMP(25);
    {
        while (!__all(pv == tag) && ++spins <= SPIN_CAP) pv = __hip_atomic_load(fq, RLX_AGENT);
        STAMP(26);
        __builtin_amdgcn_fence(__ATOMIC_ACQUIRE, "wavefront");
        u32x4 t[4];
#pragma unroll
        for (int i = 0; i < 4; i++) t[i] = __builtin_amdgcn_raw_buffer_load_b128(bws, l16 + i * 1024, WS_O + (u32)(og * 32 + wv * 4) * 1024, 16);
        RBAR();
#pragma unroll
        for (int i = 0; i < 4; i++) lds_w128(LB + L_OST + ((wv * 4 + i) ^ ((rb & 7) * 2)) * 1024 + l16, t[i]);
    }
    STAMP(27);
    SB(); BAR();
    STAMP(28);
    constexpr u32 TP = 528;
    {
        u32x4 of[16];
#pragma unroll
        for (int s = 0; s < 16; s++) of[s] = lds_r128(LB + L_OST + (kh * 16 + s) * 1024 + l16);
        SB();
#pragma unroll
        for (int s = 0; s < 16; s++) acc = mfma32(wof[s], of[s], s ? acc : zero);
        const u32 a = LB + L_TR + kh * 32 * TP + l31 * TP + cw * 128 + h * 16;
#pragma unroll
        for (int q = 0; q < 4; q++) lds_wf4(a + q * 32, Q4(acc, q));
    }
    for (u32 mm = mine; mm; mm &= mm - 1) {
        const int tk = __builtin_ctz(mm);
        const u32 yo = WS_Y + (u32)((tk * 8 + (hq >> 1) * 2 + (tid >> 8)) * 28 + ((tid >> 5) & 7)) * 512 + (tid & 31) * 16;
        const bool last = ((tid >> 5) & 7) < 4;
        u32x4 t[4];
        for (;;) {
#pragma unroll
            for (int i = 0; i < 4; i++) t[i] = __builtin_amdgcn_raw_buffer_load_b128(bws, (i < 3 || last) ? yo + i * 4096 : yo, 0, 16);
            u32 d = 0;
#pragma unroll
            for (int i = 0; i < 4; i++) d |= (t[i].y ^ tag) | (t[i].w ^ tag);
            asm volatile("" ::: "memory");
            if (__all(d == 0u) || ++spins > SPIN_CAP) break;
        }
        const float y0 = (__uint_as_float(t[0].x) + __uint_as_float(t[1].x)) + (__uint_as_float(t[2].x) + (last ? __uint_as_float(t[3].x) : 0.f));
        const float y1 = (__uint_as_float(t[0].z) + __uint_as_float(t[1].z)) + (__uint_as_float(t[2].z) + (last ? __uint_as_float(t[3].z) : 0.f));
        *(LAS f32x2*)(LB + L_YR + tk * 4096 + ((tid >> 5) & 7) * 512 + (tid >> 8) * 256 + (tid & 31) * 8) = (f32x2){y0, y1};
    }
    STAMP(29);
    SB(); BAR();
    STAMP(30);
    for (u32 mm = mine; mm; mm &= mm - 1) {
        const int tk = __builtin_ctz(mm);
        const u32 key = __builtin_amdgcn_readlane(selk, tk);
        if (2047 - (int)(key & 2047u) == orow) {
            f32x4 y0 = bc4(0.f), y1 = bc4(0.f);
#pragma unroll
            for (int c = 0; c < 8; c++) { y0 += lds_rf4(LB + L_YR + tk * 4096 + c * 512 + (tid & 15) * 32); y1 += lds_rf4(LB + L_YR + tk * 4096 + c * 512 + (tid & 15) * 32 + 16); }
            const u32 k16 = key >> 11, rbits = (k16 & 0x8000u) ? (k16 & 0x7fffu) : (~k16 & 0xffffu);
            const float rsel = rbf(__builtin_amdgcn_rcpf(1.0f + __builtin_amdgcn_exp2f(bf_lo(rbits) * -1.4426950408889634f)));
            const u32x4 yp = pack8(y0, y1);
#pragma unroll
            for (int e = 0; e < 4; e++) {
                const u32 yy = pk_bf16(bf_lo(yp[e]) * rsel, bf_hi(yp[e]) * rsel);
                const u32 xb = pk_bf16(bf_lo(xnp[e]) + bf_lo(yy), bf_hi(xnp[e]) + bf_hi(yy));
                gp[e] = pk_bf16(silu_(bf_lo(xb)), silu_(bf_hi(xb)));
            }
        }
    }
    {
        const u32 a = LB + L_TR + (tid >> 4) * TP + (tid & 15) * 32;
        const u32x4 sp4 = pack8(lds_rf4(a) + lds_rf4(a + 32 * TP), lds_rf4(a + 16) + lds_rf4(a + 32 * TP + 16));
        u32x4 ov;
#pragma unroll
        for (int e = 0; e < 4; e++) {
            const u32 t = pk_bf16(bf_lo(sp4[e]) * bf_lo(gp[e]), bf_hi(sp4[e]) * bf_hi(gp[e]));
            ov[e] = pk_bf16(bf_lo(xe[e]) + bf_lo(t), bf_hi(xe[e]) + bf_hi(t));
        }
        *(GAS u32x4*)(out + (size_t)orow * 512 + ocol) = ov;
    }
    if (b == 0 && wv == 3) {
        for (;;) { bool ok = true;
#pragma unroll
            for (int i = 0; i < 4; i++) { const int w = lane + 64 * i; ok &= __hip_atomic_load((const GAS u32*)(ws + WS_FLAGK + ((w >> 3) * 2 * 16 + (w & 7) * 2) * FS), RLX_AGENT) == tag; }
            if (__all(ok) || ++spins > SPIN_CAP) break;
            __builtin_amdgcn_s_sleep(2);
        }
        if (lane == 0) __hip_atomic_store((GAS u32*)(ws + WS_EPOCH), tag, RLX_AGENT);
    }
}
#define STAMP_WAVES 8
#define STAMP_NAMES {"start","pre/L:x0 in","P0 ok/L:Wk in","L:Wv in","L:Wq in","K mfma done/S:Kfl","K stored/S:V iss","flagK/R:gath","V stored/R:sel","flagV/R:A1","K issued","Q done","rel PQ","diag done","V issued","S23 done","S1 done","F1 done","","F done","O ok","o stored","flagO","rel Bw","arr Bw","wof","o poll ok","arr Bo","rel Bo","arr Bs","rel Bs"}

extern "C" void kernel_launch(void* const* d_in, const int* in_sizes, int n_in, void* d_out, int out_size, void* d_ws, size_t ws_size, hipStream_t stream) {
    fused_kernel<<<256, 512, 0, stream>>>((const u16*)d_in[0], (const u16*)d_in[1], (char*)d_ws, (const u16*)d_in[2], (u16*)d_out, (const u16*)d_in[4], (const u16*)d_in[5], (const u16*)d_in[6], (const u16*)d_in[7]);
}
```

```cpp
#include <hip/hip_runtime.h>
#include <stdint.h>

typedef unsigned short u16;
typedef unsigned u32;
typedef unsigned long long u64;
typedef u32 u32x4 __attribute__((ext_vector_type(4)));
typedef u32 u32x2 __attribute__((ext_vector_type(2)));
typedef int i32x4 __attribute__((ext_vector_type(4)));
typedef __bf16 bf16x8 __attribute__((ext_vector_type(8)));
typedef __bf16 bf16x4 __attribute__((ext_vector_type(4)));
typedef __bf16 bf16x2 __attribute__((ext_vector_type(2)));
typedef float f32x16 __attribute__((ext_vector_type(16)));
typedef float f32x4 __attribute__((ext_vector_type(4)));
typedef float f32x2 __attribute__((ext_vector_type(2)));
#define LAS __attribute__((address_space(3)))
#define GAS __attribute__((address_space(1)))
#define DI __device__ __forceinline__
#define RLX_AGENT __ATOMIC_RELAXED, __HIP_MEMORY_SCOPE_AGENT

constexpr u32 WS_EPOCH = 0;
constexpr u32 FS = 64;
constexpr u32 WS_FLAGK = 128 << 10;
constexpr u32 WS_FLAGV = 256 << 10;
constexpr u32 WS_FLAGO = 384 << 10;
constexpr u32 WS_ONES = 512 << 10;
constexpr u32 WS_R = 64 << 10;
constexpr u32 WS_S = 80 << 10;
constexpr u32 WS_FGU = 96 << 10;
constexpr u32 WS_GU = 7 << 20;
constexpr u32 WS_Y = 8 << 20;
constexpr u32 WS_K = 1 << 20;
constexpr u32 WS_V = 3 << 20;
constexpr u32 WS_O = 5 << 20;
constexpr u32 WS_USED = 9 << 20;
#define STAMP(i) do {} while (0)

#define STAMPS_STEP(n) do {} while (0)
constexpr u32 SL = 8192, GR = 8 * SL;
constexpr u32 L_SCR = 2 * GR;
constexpr u32 L_EXQ = L_SCR;
constexpr u32 EXS = 4608;
constexpr u32 L_OWN = L_SCR + 18432;
constexpr u32 L_STP = L_SCR + 26624;
constexpr u32 L_H = L_STP;
constexpr u32 L_CS = L_SCR + 27648;
constexpr u32 L_ST = L_SCR + 29184;
constexpr u32 L_FLG = L_SCR + 29696;
constexpr u32 L_SEL = L_SCR + 29952;
constexpr u32 L_ONES = L_SCR + 30720;
constexpr u32 L_FW = L_SCR;
constexpr u32 L_OST = 65536;
constexpr u32 L_TR = 0;
constexpr u32 L_YR = 98304;
constexpr u32 L_TOTAL = 163840;
__device__ constexpr u32 fill_slot(int f) { return f < 4 ? 4 + f : ((f - 4) >> 1) + ((f & 1) ? 4 : 0); }
constexpr u32 F_CS = 0;
constexpr u32 F_CSV = 1;
constexpr u32 F_LAND = 21;
constexpr u32 F_FW = 24;
constexpr u32 F_HALO = 2;
constexpr u32 F_MW = 3;
constexpr u32 F_VREQ = 4;
constexpr u32 F_WO = 5;
constexpr u32 F_WOF = 6;
constexpr u32 F_SEL = 7;
constexpr u32 F_Q = 8;
constexpr u32 F_O = 12;
constexpr u32 F_OST = 16;
constexpr u32 F_H = 20;
constexpr u32 F_TAG = 25;

DI float bf_lo(u32 v) { return __uint_as_float(v << 16); }
DI float bf_hi(u32 v) { return __uint_as_float(v & 0xffff0000u); }
DI u32 pk_bf16(float a, float b) { f32x2 f = {a, b}; return __builtin_bit_cast(u32, __builtin_convertvector(f, bf16x2)); }
DI float rbf(float a) { return bf_lo(pk_bf16(a, a)); }
DI float dot2(u32 a, u32 b, float c) { return __builtin_amdgcn_fdot2_f32_bf16(__builtin_bit_cast(bf16x2, a), __builtin_bit_cast(bf16x2, b), c, false); }
DI f32x16 mfma32(u32x4 a, u32x4 b, f32x16 c) { return __builtin_amdgcn_mfma_f32_32x32x16_bf16(__builtin_bit_cast(bf16x8, a), __builtin_bit_cast(bf16x8, b), c, 0, 0, 0); }
DI f32x4 mfma16(u32x4 a, u32x4 b, f32x4 c) { return __builtin_amdgcn_mfma_f32_16x16x32_bf16(__builtin_bit_cast(bf16x8, a), __builtin_bit_cast(bf16x8, b), c, 0, 0, 0); }
DI u32x2 tr_read(u32 addr) { return __builtin_bit_cast(u32x2, __builtin_amdgcn_ds_read_tr16_b64_v4bf16((LAS bf16x4*)addr)); }
DI u32x4 lds_r128(u32 addr) { return *(LAS u32x4*)addr; }
DI f32x4 lds_rf4(u32 addr) { return *(LAS f32x4*)addr; }
DI void lds_w128(u32 addr, u32x4 v) { *(LAS u32x4*)addr = v; }
DI void lds_wf4(u32 addr, f32x4 v) { *(LAS f32x4*)addr = v; }
template <int CTRL> DI float dpp_add(float v) { return v + __uint_as_float(__builtin_amdgcn_update_dpp(0u, __float_as_uint(v), CTRL, 0xf, 0xf, true)); }
DI float red8(float v) { v = dpp_add<0xB1>(v); v = dpp_add<0x4E>(v); return dpp_add<0x141>(v); }
DI i32x4 mk_rsrc(const void* p, u32 bytes) { u64 a = (u64)p; i32x4 r; r.x = (int)(u32)a; r.y = (int)(u32)(a >> 32) & 0xffff; r.z = (int)bytes; r.w = 0x00020000; return r; }
DI void dma16(u32 lds, u32 voff, i32x4 rsrc, u32 soff) {
    asm volatile("s_mov_b32 m0, %0\n\ts_nop 0\n\tbuffer_load_dwordx4 %1, %2, %3 offen lds" :: "s"(lds), "v"(voff), "s"(rsrc), "s"(soff) : "memory");
}
DI void touch(u32 lds, const GAS void* p) { asm volatile("s_mov_b32 m0, %0\n\ts_nop 0\n\tglobal_load_lds_dword %1, off" :: "s"(lds), "v"(p) : "memory"); }
DI void dma16c(u32 lds, u32 voff, i32x4 rsrc, u32 soff) {
    asm volatile("s_mov_b32 m0, %0\n\ts_nop 0\n\tbuffer_load_dwordx4 %1, %2, %3 offen sc1 lds" :: "s"(lds), "v"(voff), "s"(rsrc), "s"(soff) : "memory");
}
DI u32 opaque(u32 v) { asm volatile("" : "+v"(v)); return v; }
DI u32x4 pack8(f32x4 a, f32x4 b) { return (u32x4){pk_bf16(a.x, a.y), pk_bf16(a.z, a.w), pk_bf16(b.x, b.y), pk_bf16(b.z, b.w)}; }
#define Q4(acc, q) ((f32x4){acc[4 * (q)], acc[4 * (q) + 1], acc[4 * (q) + 2], acc[4 * (q) + 3]})
#define BAR() asm volatile("s_waitcnt lgkmcnt(0)\n\ts_barrier" ::: "memory")
#define RBAR() asm volatile("s_barrier" ::: "memory")
#define WAITV_(n) asm volatile("s_waitcnt vmcnt(" #n ")" ::: "memory")
#define WAITV(n) ({ WAITV_(n); })
constexpr u32 SPIN_CAP = 1u << 20;
#define XDELAY 6
#define WOEARLY 0
#define FORCE_FB 0
#define WARM 0
#define CODE_LO 0
#define CODE_N 3
#define OUTAUX 16
#define WO1 2
#define FORCE_SEL 0
#define ROTX 2
#define WOQ 4
constexpr u32 ONES = 0x3f803f80u;

#define SB() __builtin_amdgcn_sched_barrier(0)
DI float np(float r) { asm("" : "+v"(r)); return r; }
DI u32x2 scl4(f32x4 a, f32x4 r) { return (u32x2){pk_bf16(np(a.x * r.x), np(a.y * r.y)), pk_bf16(np(a.z * r.z), np(a.w * r.w))}; }
DI u32x4 split3(float c) {
    const u32 a = pk_bf16(c, c); const float r1 = np(c - bf_lo(a));
    const u32 b = pk_bf16(r1, r1); const float r2 = np(r1 - bf_lo(b));
    return (u32x4){(a & 0xffffu) | (b & 0xffff0000u), pk_bf16(r2, 0.f), 0u, 0u};
}
DI f32x4 addmul4(f32x4 a, f32x4 b, float m) { return (f32x4){np(np(a.x + b.x) * m), np(np(a.y + b.y) * m), np(np(a.z + b.z) * m), np(np(a.w + b.w) * m)}; }
DI f32x4 add4(f32x4 a, f32x4 b) { return (f32x4){np(a.x + b.x), np(a.y + b.y), np(a.z + b.z), np(a.w + b.w)}; }
DI f32x4 bc4(float a) { return (f32x4){a, a, a, a}; }
DI u32x4 cat(u32x2 a, u32x2 b) { return (u32x4){a.x, a.y, b.x, b.y}; }
#define PIN4(v) asm volatile("" : "+v"(v))
DI void lds_sig_(u32& pt, u32 addr) {
    asm volatile("v_mov_b32 %0, %1\n\ts_mov_b64 exec, 1\n\tds_add_u32 %0, %0\n\ts_mov_b64 exec, -1" : "+v"(pt) : "s"(addr) : "memory");
}
DI void lds_need_(u32& pt, u32& have, u32 addr, u32 n) {
    u32 c;
    asm volatile("s_cmp_ge_u32 %0, %4\n\ts_cbranch_scc1 2f\n\ts_mov_b32 %2, 0x400000\n"
                 "1:\n\tv_mov_b32 %1, %3\n\tds_read_b32 %1, %1\n\ts_waitcnt lgkmcnt(0)\n\tv_readfirstlane_b32 %0, %1\n\ts_cmp_ge_u32 %0, %4\n\ts_cbranch_scc1 2f\n\ts_sub_u32 %2, %2, 1\n\ts_cbranch_scc0 1b\n2:"
                 : "+s"(have), "+v"(pt), "=&s"(c) : "s"(addr), "s"(n) : "memory", "scc");
}
DI void lds_cnt_(u32& pt, u32 addr, u32 n) {
    u32 c, v;
    asm volatile("s_mov_b32 %2, 0x400000\n"
                 "1:\n\tv_mov_b32 %1, %3\n\tds_read_b32 %1, %1\n\ts_waitcnt lgkmcnt(0)\n\tv_readfirstlane_b32 %0, %1\n\ts_cmp_ge_u32 %0, %4\n\ts_cbranch_scc1 2f\n\ts_sub_u32 %2, %2, 1\n\ts_cbranch_scc0 1b\n2:"
                 : "=&s"(v), "+v"(pt), "=&s"(c) : "s"(addr), "s"(n * addr) : "memory", "scc");
}
DI void lds_bits_(u32& pt, u32 addr, u32 bits) {
    u32 c, v;
    asm volatile("s_mov_b32 %2, 0x400000\n"
                 "1:\n\tv_mov_b32 %1, %3\n\tds_read_b32 %1, %1\n\ts_waitcnt lgkmcnt(0)\n\tv_readfirstlane_b32 %0, %1\n\ts_andn2_b32 %0, %4, %0\n\ts_cbranch_scc0 2f\n\ts_sub_u32 %2, %2, 1\n\ts_cbranch_scc0 1b\n2:"
                 : "=&s"(v), "+v"(pt), "=&s"(c) : "s"(addr), "s"(bits) : "memory", "scc");
}
#define lds_sig(a) lds_sig_(pt, a)
#define lds_need(h, a, n) lds_need_(pt, h, a, n)
#define lds_cnt(a, n) lds_cnt_(pt, a, n)
#define lds_bits(a, b) lds_bits_(pt, a, b)
DI f32x16 mfp(u32x4 a, u32x4 b, f32x16 c) { f32x16 r = mfma32(a, b, c); PIN4(r); return r; }
DI u32 pkp(float a, float b) { return opaque(pk_bf16(a, b)); }
template <int CTRL, int RM> DI u32 dpp_umax(u32 v) { const u32 o = __builtin_amdgcn_update_dpp(0u, v, CTRL, RM, 0xf, false); return v > o ? v : o; }
DI u32 wave_umax(u32 v) { v = dpp_umax<0x111, 0xf>(v); v = dpp_umax<0x112, 0xf>(v); v = dpp_umax<0x114, 0xf>(v); v = dpp_umax<0x118, 0xf>(v); v = dpp_umax<0x142, 0xa>(v); v = dpp_umax<0x143, 0xc>(v);
    return __builtin_amdgcn_readlane(v, 63); }
DI float silu_(float x) { return x * __builtin_amdgcn_rcpf(1.0f + __builtin_amdgcn_exp2f(x * -1.4426950408889634f)); }
DI u32 norm2(u32 x, float m, float rs) { const u32 d = pk_bf16(bf_lo(x) - m, bf_hi(x) - m); return pk_bf16(bf_lo(d) * rs, bf_hi(d) * rs); }
DI void st_sc1(__amdgpu_buffer_rsrc_t r, u32 off, u32x4 v) { __builtin_amdgcn_raw_buffer_store_b128(v, r, off, 0, 16); }
DI float swap_add(float m) { auto sw = __builtin_amdgcn_permlane32_swap(__float_as_uint(m), __float_as_uint(m), false, false); return __uint_as_float(sw[0]) + __uint_as_float(sw[1]); }
DI float lds_rf(u32 a) { return *(LAS float*)a; }
DI void lds_wf(u32 a, float v) { *(LAS float*)a = v; }

__global__ __launch_bounds__(512, 2) void fused_kernel(const u16* __restrict__ x, const u16* __restrict__ wqkv, char* __restrict__ ws, const u16* __restrict__ wo, u16* __restrict__ out,
                                                        const u16* __restrict__ wr, const u16* __restrict__ wg, const u16* __restrict__ wu, const u16* __restrict__ wd) {
    asm volatile("" :: "s"(x), "s"(wqkv), "s"(ws), "s"(wo), "s"(out), "s"(wr), "s"(wg), "s"(wu), "s"(wd));
    __shared__ __attribute__((aligned(1024))) char lds_mem[L_TOTAL];
    const u32 LB = (u32)(uintptr_t)lds_mem;
    int b = blockIdx.x, tid = threadIdx.x;
    const int wv = __builtin_amdgcn_readfirstlane(tid >> 6);
    if (wv < 2) {
        const int rb = ((b & 7) >> 1) * 8 + (b >> 5), hq = (b & 1) * 4 + ((b >> 3) & 3), rot = (rb + ROTX * hq) & 3, lane = tid & 63;
        const i32x4 rx = mk_rsrc(x, 2048 * 512 * 2), rw = mk_rsrc(wqkv, 512 * 1536 * 2);
        const u32 vx0 = (u32)(lane >> 3) * 1024 + (((lane & 7) ^ ((lane >> 4) & 3)) << 4), vx1 = vx0 ^ 64;
        u32 so = (u32)(rb * 64) * 1024 + (u32)rot * 256 + wv * 128, ld = LB + wv * GR;
#pragma unroll
        for (int j = 0; j < 8; j++) { dma16(ld, (j & 1) ? vx1 : vx0, rx, so); ld += 1024; so += 8 * 1024; }
        const u32 vw = (u32)(lane >> 3) * 3072 + (((lane & 7) ^ (((lane >> 4) & 1) << 2)) << 4);
        so = ((u32)rot * 128 + wv * 64) * 3072 + 1024 + hq * 128; ld = LB + wv * GR + 4 * SL;
#pragma unroll
        for (int j = 0; j < 8; j++) { dma16(ld, vw, rw, so); ld += 1024; so += 8 * 3072; }
    }
    asm volatile("" : "+s"(b), "+v"(tid));
    const int xcd = b & 7, bi = b >> 3;
    const int rb = (xcd >> 1) * 8 + (bi >> 2), hq = (xcd & 1) * 4 + (bi & 3);
    const int lane = tid & 63;
    const int rot = (rb + ROTX * hq) & 3;
    const int h = lane >> 5, l31 = lane & 31;
#define STAMP_OUT() do {} while (0)

    const u32 flg = LB + L_FLG;
    const i32x4 rs_ws = mk_rsrc(ws, WS_USED);
    const __amdgpu_buffer_rsrc_t bws = __builtin_amdgcn_make_buffer_rsrc(ws, 0, WS_USED, 0x00020000);
    const u32 l16 = opaque(lane * 16);
    u32 spins = 0, pt = 0;
    u32 tag;
    f32x16 acc, zero;
#pragma unroll
    for (int i = 0; i < 16; i++) zero[i] = 0.f;
    const int erow = (hq & 1) * 32 + (tid >> 4);
    const int orow = rb * 64 + erow, ocol = (hq >> 1) * 128 + (tid & 15) * 8, og = rb * 2 + (hq & 1);
    u32x4 xe;
    u32 gp[4], xnp[4];
    auto calc_g = [&]() {
        const float e_m = lds_rf(LB + L_ST + 256 + erow * 4), e_rs = lds_rf(LB + L_ST + erow * 4);
#pragma unroll
        for (int e = 0; e < 4; e++) { xnp[e] = opaque(norm2(xe[e], e_m, e_rs)); gp[e] = opaque(pk_bf16(silu_(bf_lo(xnp[e])), silu_(bf_hi(xnp[e])))); }
    };
    auto ld_xe = [&]() { xe = *(const GAS u32x4*)(x + (size_t)orow * 512 + ocol); };
    auto st_flag = [&](u32 off) { __builtin_amdgcn_raw_buffer_store_b32(tag, bws, 0, off, 16); };
    auto get_tag = [&]() { tag = __builtin_amdgcn_readfirstlane(*(volatile LAS u32*)(flg + F_TAG * 4)); };
    constexpr u32 TP = 528;
    auto ffn_h = [&]() {
        if (b < 224) {
            const GAS u32* fg = (const GAS u32*)(ws + WS_FGU + ((b & ~7) + (lane & 7)) * FS);
            while (!__all(__hip_atomic_load(fg, RLX_AGENT) == tag) && ++spins <= SPIN_CAP) __builtin_amdgcn_s_sleep(4);
            __builtin_amdgcn_fence(__ATOMIC_ACQUIRE, "wavefront");
            STAMP(11);
            u32x4 gu[2][8];
#pragma unroll
            for (int j = 0; j < 2; j++)
#pragma unroll
                for (int i = 0; i < 8; i++) gu[j][i] = __builtin_amdgcn_raw_buffer_load_b128(bws, l16 + (wv - 2) * 2048 + j * 1024, WS_GU + (u32)((b & ~7) + i) * 4096, 16);
#pragma unroll
            for (int j = 0; j < 2; j++) {
                float a[4];
#pragma unroll
                for (int e = 0; e < 4; e++) { a[e] = __uint_as_float(gu[j][0][e]);
#pragma unroll
                    for (int i = 1; i < 8; i++) a[e] = np(a[e] + __uint_as_float(gu[j][i][e])); }
                const u32 gq = pk_bf16(a[0], a[2]), uq = pk_bf16(a[1], a[3]), sq = pk_bf16(silu_(bf_lo(gq)), silu_(bf_hi(gq)));
                const u32 hp = pk_bf16(bf_lo(sq) * bf_lo(uq), bf_hi(sq) * bf_hi(uq));
                const u32 ha = LB + L_H + (lane & 3) * 256 + (((wv - 2) * 2 + j) * 16 + (lane >> 2)) * 2;
                *(LAS u16*)ha = (u16)hp; *(LAS u16*)(ha + 128) = (u16)(hp >> 16);
            }
            lds_sig(flg + F_H * 4);
        }
    };
    auto gather_o = [&]() {
        lds_cnt(flg + F_WOF * 4, 4);
        calc_g();
        const u32 x2 = (rb & 7) * 2;
        const GAS u32* fq = (const GAS u32*)(ws + WS_FLAGO + (og * 16 + (((wv * 8 + (lane & 3) * 2) ^ x2) >> 1)) * FS);
        while (!__all(__hip_atomic_load(fq, RLX_AGENT) == tag) && ++spins <= SPIN_CAP) {}
        STAMP(26);
#pragma unroll
        for (int i = 0; i < 8; i++) dma16c(LB + L_OST + (wv * 8 + i) * 1024, l16, rs_ws, WS_O + (u32)(og * 32 + ((wv * 8 + i) ^ x2)) * 1024);
        WAITV(0); lds_sig(flg + (F_OST + wv) * 4);
        STAMP(27);
        lds_cnt(flg + F_SEL * 4, 1);
        const u32 selk = *(LAS u32*)(LB + L_SEL + (lane & 7) * 4);
        for (u32 mine = (u32)__ballot(((2047u - (selk & 2047u)) >> 5) == (u32)og) & 0xffu; mine; mine &= mine - 1) {
            const int tk = __builtin_ctz(mine);
            const u32 yo = (u32)((tk * 8 + (hq >> 1) * 2 + h) * 28 + wv * 7) * 512 + l31 * 16;
            float y0, y1;
            for (;;) {
                u32x4 t[7];
#pragma unroll
                for (int i = 0; i < 7; i++) t[i] = __builtin_amdgcn_raw_buffer_load_b128(bws, yo + i * 512, WS_Y, 16);
                u32 d = 0; y0 = 0; y1 = 0;
#pragma unroll
                for (int i = 0; i < 7; i++) { d |= (t[i].y ^ tag) | (t[i].w ^ tag); y0 += __uint_as_float(t[i].x); y1 += __uint_as_float(t[i].z); }
                asm volatile("" ::: "memory");
                if (__all(d == 0u) || ++spins > SPIN_CAP) break;
            }
            *(LAS f32x2*)(LB + L_YR + tk * 2048 + wv * 512 + lane * 8) = (f32x2){y0, y1};
        }
        STAMP(24);
    };

    if (__builtin_expect(wv < 2, 1)) {
            __builtin_amdgcn_s_setprio(3);
            const int lw = wv;
            const u32 GB = LB + lw * GR;
            const i32x4 rx = mk_rsrc(x, 2048 * 512 * 2), rw = mk_rsrc(wqkv, 512 * 1536 * 2);
            const u32 vx0 = (u32)(lane >> 3) * 1024 + (((lane & 7) ^ ((lane >> 4) & 3)) << 4), vx1 = vx0 ^ 64;
            auto fill_x = [&](int bk) {
                u32 so = (u32)(rb * 64) * 1024 + (u32)((bk + rot) & 3) * 256 + lw * 128, ld = GB + bk * SL;
#pragma unroll
                for (int j = 0; j < 8; j++) { dma16(ld, (j & 1) ? vx1 : vx0, rx, so); ld += 1024; so += 8 * 1024; }
            };
            const u32 vw = (u32)(lane >> 3) * 3072 + (((lane & 7) ^ (((lane >> 4) & 1) << 2)) << 4);
            auto fill = [&](int f) {
                const int seg = (f >> 2) == 0 ? 1 : (f >> 2) == 1 ? 2 : 0;
                u32 so = ((u32)((f + rot) & 3) * 128 + lw * 64) * 3072 + seg * 1024 + hq * 128;
                u32 ld = GB + fill_slot(f) * SL;
#pragma unroll
                for (int j = 0; j < 8; j++) { dma16(ld, vw, rw, so); ld += 1024; so += 8 * 3072; }
            };
            fill_x(1); fill(1);
            WAITV(16); STAMP(1); RBAR();
            fill_x(2); fill(2);
            WAITV(16); RBAR();
            fill_x(3); fill(3);
            WAITV(16); RBAR();
            fill(4); fill(5);
            WAITV(16); STAMP(2); RBAR();
            fill(6);
            WAITV(16); RBAR();
            fill(7);
            WAITV(16); RBAR();
            fill(8);
            WAITV(8); STAMP(3); lds_sig(flg + F_LAND * 4);
            fill(9); fill(10);
            WAITV(8); lds_sig(flg + (F_LAND + 1) * 4);
            fill(11);
            WAITV(0); STAMP(4); lds_sig(flg + (F_LAND + 2) * 4);
            auto fill_o = [&](int sl) {
                const i32x4 ro = mk_rsrc(wo, 512 * 512 * 2);
                const u32 vo_w = (u32)(lane >> 4) * 1024 + (((lane & 15) ^ (((lane >> 4) & 3) << 2)) << 4);
                u32 so = (u32)(sl ^ (rb & 7)) * 32768 + (hq >> 1) * 256, ld = LB + sl * SL;
#pragma unroll
                for (int j = 0; j < 8; j++) { dma16(ld, vo_w, ro, so); ld += 1024; so += 4096; }
            };
            get_tag(); ld_xe();
            lds_cnt(flg + F_CSV * 4, 4);
            if (lw) {
#pragma unroll 1
                for (int i = 0; i < 2 * WO1; i++) { fill_o((i & 1) * 8 + (i & 2) * 2 + (i >> 2)); WAITV(WOQ); }
            } else {
                const int gg = rb * 2 - 8 + (lane & 7);
                const GAS u32* fp = (const GAS u32*)(ws + ((lane & 16) ? WS_FLAGV : WS_FLAGK) + ((gg < 0 ? 0 : gg) * 16 + hq * 2 + ((lane >> 3) & 1)) * FS);
                for (;;) {
                    const u32 ok = (u32)__ballot(gg < 0 || __hip_atomic_load(fp, RLX_AGENT) == tag);
                    const bool done = ok == 0xffffffffu || ++spins > SPIN_CAP;
                    if (lane == 0) *(volatile LAS u32*)(flg + F_HALO * 4) = done ? 0xffffffffu : ok;
                    if (done) break;
                }
                STAMP(7);
            }
            lds_cnt(flg + F_VREQ * 4, 4);
            STAMP(5);
#pragma unroll 1
            for (int i = WO1; i < 8; i++) { fill_o(lw * 8 + (i & 1) * 4 + (i >> 1)); WAITV(8); }
            WAITV(0); lds_sig(flg + F_WO * 4);
            STAMP(6);
            gather_o();
    } else if (__builtin_expect(wv >= 4, 1)) {
    __builtin_amdgcn_s_setprio(2);
    const int cw = wv - 4, rg = cw & 1, pr = cw >> 1;
    const int g = rb * 2 + rg, pair = hq * 2 + pr, g0 = rb * 2 - 8 + rg * 4;
    u32 xfa[2][4], wfa[2][4];
    {
        const int row = rg * 32 + l31;
        const u32 w0 = LB + (u32)(8 * h + ((lane & 15) >> 2)) * 128 + ((pr ^ ((lane >> 3) & 1)) << 6) + (((lane >> 4) & 1) << 5) + ((lane & 3) << 3);
#pragma unroll
        for (int s = 0; s < 4; s++) {
            xfa[0][s] = opaque(LB + (row >> 3) * 1024 + (row & 7) * 128 + (((2 * (s ^ cw) + h) ^ ((row >> 1) & 7)) << 4)); xfa[1][s] = opaque(xfa[0][s] + GR);
            wfa[0][s] = opaque(w0 + (s ^ cw) * 2048); wfa[1][s] = opaque(w0 + (s ^ cw) * 2048 + GR);
        }
    }
    const u32 csw = opaque(LB + L_CS + (rg * 64 + pr * 32 + ((lane & 1) << 4) + (lane >> 4) * 4) * 4), csr = opaque(LB + L_CS + (h * 64 + pr * 32 + l31) * 4);
    u32 hso[4];
#pragma unroll
    for (int t = 0; t < 4; t++) { hso[t] = g0 + t >= 0 ? (u32)((g0 + t) * 32 + pair * 2) * 1024 : 0x40000000u; asm volatile("" : "+s"(hso[t])); }
    u32 dmask[8];
    {
        const u32 mbits = (u32)((2ull << l31) - 1ull) >> (4 * h);
#pragma unroll
        for (int i = 0; i < 8; i++) { const int k = ((2 * i) & 3) + 8 * (i >> 1);
            dmask[i] = opaque(((u32)__builtin_amdgcn_sbfe(mbits, k, 1) & 0xffffu) | ((u32)__builtin_amdgcn_sbfe(mbits, k + 1, 1) & 0xffff0000u)); }
    }
    u32 own = rg ? LB + L_EXQ + cw * EXS : LB + L_OWN + pr * 4096;
    asm volatile("" : "+s"(own));
    const u32 vlo = h * 256 + (lane & 15) * 16;
    const u32 vab = opaque((lane & 16) ? WS_ONES + b * 2048 : WS_V + vlo), vam = opaque((lane & 16) ? 0x40000000u : 0xffffffffu);
    const u32 ownv = opaque((lane & 16) ? LB + L_ONES : own + 2048 + vlo);
    const u32 kbits = 0xfu << (pr * 8 + rg * 4), vbits = kbits << 16;
    const u32 selv = opaque((((lane >> 4) ^ lane) & 1) ? 0u : ONES);
    const u32x4 sel = {selv, selv, selv, selv};
    u32 blk = (u32)(g * 32 + pair * 2) * 1024, fo = (u32)(g * 16 + pair) * FS;
    asm volatile("" : "+s"(blk), "+s"(fo));
    const u32 vst = opaque(((lane >> 4) & 1) * 1024 + vlo);
    const u32 sta = opaque(LB + L_STP + (rg * 32 + l31) * 16);
    u32x4 xf[8][4], wf[2][4];
    f32x16 acc2;
    f32x4 cs;
    float sx0 = 0, sx1 = 0, sq0 = 0, sq1 = 0;
    auto rw_ = [&](int n, int s) {
        const u32 a = wfa[n & 1][s] + fill_slot(n >> 1) * SL;
        const u32x2 lo = tr_read(a), hi = tr_read(a + 512); wf[n & 1][s] = (u32x4){lo.x, lo.y, hi.x, hi.y};
        if (n < 8) xf[n][s] = lds_r128(xfa[n & 1][s] + (n >> 1) * SL);
    };
    auto mm_ = [&](int n, int s, f32x16& a) {
        const f32x16 c = ((n & 7) == 0 && s == 0) ? zero : a;
        a = (n >> 3) != 1 ? mfma32(wf[n & 1][s], xf[n & 7][s], c) : mfma32(xf[n & 7][s], wf[n & 1][s], c);
        PIN4(a);
    };
    auto cs_ = [&](int n, int s) { cs = mfma16(wf[n & 1][s], sel, ((n & 7) == 0 && s == 0) ? bc4(0.f) : cs); PIN4(cs); };
    auto st_ = [&](int i) {
#pragma unroll
        for (int s = 0; s < 2; s++) { const u32x4 v = xf[i][s];
            sx0 = dot2(v.x, ONES, sx0); sq0 = dot2(v.x, v.x, sq0); sx1 = dot2(v.y, ONES, sx1); sq1 = dot2(v.y, v.y, sq1);
            sx0 = dot2(v.z, ONES, sx0); sq0 = dot2(v.z, v.z, sq0); sx1 = dot2(v.w, ONES, sx1); sq1 = dot2(v.w, v.w, sq1); }
        asm volatile("" : "+v"(sx0), "+v"(sx1), "+v"(sq0), "+v"(sq1));
    };
#define RW(n, s) do { if ((n) < 24) rw_(n, s); } while (0)
#define STEP(n, A, X0, X1, X2, X3) do { STAMPS_STEP(n); \
        mm_(n, 0, A); SB(); RW(n + 1, 0); X0; SB(); cs_(n, 0); SB(); \
        mm_(n, 1, A); SB(); RW(n + 1, 1); X1; SB(); \
        mm_(n, 2, A); SB(); RW(n + 1, 2); X2; SB(); cs_(n, 2); SB(); \
        mm_(n, 3, A); SB(); RW(n + 1, 3); X3; SB(); if ((n) < 8) st_(n); if (!((n) & 1) && (n) <= 8) { if ((n) == 8) asm volatile("s_waitcnt lgkmcnt(8)" ::: "memory"); RBAR(); } SB(); } while (0)
#define NOP_ do {} while (0)
#define STEP0(n, A) STEP(n, A, NOP_, NOP_, NOP_, NOP_)
    u32 pk_;
#define PEEK(f) (pk_ = *(volatile LAS u32*)(flg + (f) * 4))
#define CHECK(f, n) ({ u32 hv_ = __builtin_amdgcn_readfirstlane(pk_); lds_need(hv_, flg + (f) * 4, (n) * (flg + (f) * 4)); })
    STAMP(1);
    RBAR();
    STAMP(2);
#pragma unroll
    for (int s = 0; s < 4; s++) rw_(0, s);
    SB();
    STEP0(0, acc); STEP0(1, acc); STEP0(2, acc); STEP0(3, acc); STEP0(4, acc); STEP0(5, acc); STEP0(6, acc); STEP0(7, acc);
    STAMP(5);
    {
        auto sw = __builtin_amdgcn_permlane32_swap(__float_as_uint(sx0 + sx1), __float_as_uint(sq0 + sq1), false, false);
        lds_wf(sta + h * 8 + pr * 4, __uint_as_float(sw[0]) + __uint_as_float(sw[1]));
        lds_wf4(csw, cs);
    }
    SB();
    float rs_t;
    u32x4 mfr;
    u32x4 kA, kB;
    {
        f32x4 sp; float mean, var, sd, c; u32 mp; u32x4 cf; u32x2 k0, k1;
        STEP0(8, acc2);
        sp = lds_rf4(sta); c = lds_rf(csr); SB();
        STEP(9, acc2, (mean = np(np(sp.x + sp.y) * (-1.0f / 512.0f)), mp = pk_bf16(mean, mean), mfr = (u32x4){mp, mp & 0xffffu, 0u, 0u}), (cf = split3(c)),
             (acc = mfma32(cf, mfr, acc), var = np(np(sp.z + sp.w) * (1.0f / 512.0f)), var = rbf(fmaxf(var - mean * mean, 0.f))),
             (sd = rbf(__builtin_amdgcn_sqrtf(rbf(var + 1e-5f))), rs_t = __builtin_amdgcn_rcpf(sd)));
        STEP(10, acc2, (PEEK(F_LAND), k0 = scl4(Q4(acc, 0), bc4(rs_t))), (k1 = scl4(Q4(acc, 1), bc4(rs_t)), kA = cat(k0, k1), st_sc1(bws, WS_K + blk + l16, kA)), (k0 = scl4(Q4(acc, 2), bc4(rs_t))),
             (k1 = scl4(Q4(acc, 3), bc4(rs_t)), kB = cat(k0, k1), st_sc1(bws, WS_K + 1024 + blk + l16, kB), CHECK(F_LAND, 2)));
        STAMP(6);
        STEP(11, acc2, (lds_wf(LB + L_ST + (rg * 32 + l31) * 4 + h * 256, h ? -bf_lo(mp) : rs_t)), NOP_, NOP_, NOP_);
    }
    u32 tg_;
    STEP(12, acc2, (tg_ = *(volatile LAS u32*)(flg + F_TAG * 4)), NOP_, NOP_, (tag = __builtin_amdgcn_readfirstlane(tg_))); STEP0(13, acc2);
    STEP(14, acc2, PEEK(F_LAND + 1), NOP_, NOP_, (WAITV(0), st_flag(WS_FLAGK + fo), CHECK(F_LAND + 1, 2)));
    STAMP(7);
    STEP0(15, acc2);
    lds_wf4(csw + 512, cs); lds_sig(flg + F_CSV * 4);
    SB();
    {
        const u32 sa = LB + L_ST + (rg * 32 + 4 * h) * 4;
        f32x4 r0, r1, r2, r3; float c; u32x4 cf, vS; u32x2 v0, v1;
        STEP(16, acc, NOP_, NOP_, PEEK(F_CSV), NOP_);
        CHECK(F_CSV, 4); c = lds_rf(csr + 512); SB();
        STEP(17, acc, (cf = split3(c)), (acc2 = mfma32(mfr, cf, acc2), r0 = lds_rf4(sa), r1 = lds_rf4(sa + 32)), (r2 = lds_rf4(sa + 64), r3 = lds_rf4(sa + 96), PEEK(F_MW)), (v0 = scl4(Q4(acc2, 0), r0), CHECK(F_MW, 1)));
        STEP(18, acc, (PEEK(F_LAND + 2), v1 = scl4(Q4(acc2, 1), r1)), (vS = cat(v0, v1), st_sc1(bws, WS_V + blk + vst, vS), lds_w128(own + 2048 + vst, vS)), (v0 = scl4(Q4(acc2, 2), r2)),
             (v1 = scl4(Q4(acc2, 3), r3), vS = cat(v0, v1), st_sc1(bws, WS_V + 512 + blk + vst, vS), lds_w128(own + 2048 + 512 + vst, vS), CHECK(F_LAND + 2, 2)));
    }
    STAMP(8);
    STEP0(19, acc); STEP(20, acc, NOP_, lds_w128(own + l16, kA), NOP_, lds_w128(own + 1024 + l16, kB)); STEP0(21, acc);
    STEP(22, acc, NOP_, NOP_, NOP_, (WAITV(0), st_flag(WS_FLAGV + fo)));
    STAMP(9);
    u32x4 kf[4][2], vf[4][2][2], vO[2][2];
    auto ld_ownv = [&](u32x4 (&v)[2][2]) {
#pragma unroll
        for (int j = 0; j < 4; j++) v[j >> 1][j & 1] = lds_r128(ownv + j * 512); };
    u32x4 qA, qB;
    float mA;
    {
        cs_(23, 0); cs_(23, 2); SB();
        mm_(23, 0, acc); SB(); lds_wf4(csw + 1024, cs); lds_sig(flg + F_CS * 4); SB();
        mm_(23, 1, acc); SB(); lds_bits(flg + F_HALO * 4, kbits);
#pragma unroll
        for (int hd = 0; hd < 2; hd++)
#pragma unroll
            for (int t = 0; t < 4; t++) kf[t][hd] = __builtin_amdgcn_raw_buffer_load_b128(bws, l16 + hd * 1024, WS_K + hso[t], 16);
        SB();
        STAMP(10);
        mm_(23, 2, acc); SB(); lds_cnt(flg + F_CS * 4, 4); const float c = lds_rf(csr + 1024); ld_ownv(vO); SB();
        mm_(23, 3, acc); SB();
        acc = mfma32(split3(c), mfr, acc);
        const f32x4 sF = bc4(rs_t * (0.25f * 1.4426950408889634f));
        qA = cat(scl4(Q4(acc, 0), sF), scl4(Q4(acc, 1), sF)); qB = cat(scl4(Q4(acc, 2), sF), scl4(Q4(acc, 3), sF));
        float m = dot2(qA.x, kA.x, 0.f), m2 = dot2(qB.x, kB.x, 0.f);
        m = dot2(qA.y, kA.y, m); m2 = dot2(qB.y, kB.y, m2); m = dot2(qA.z, kA.z, m); m2 = dot2(qB.z, kB.z, m2); m = dot2(qA.w, kA.w, m); m2 = dot2(qB.w, kB.w, m2);
        mA = -fmaxf(swap_add(m), swap_add(m2));
        const u32 e = LB + L_EXQ + cw * EXS;
        lds_w128(e + l16, qA); lds_w128(e + 1024 + l16, qB); lds_wf(e + 2048 + lane * 4, mA);
        lds_sig(flg + (F_Q + cw) * 4);
    }
    STAMP(11);
    f32x16 cm;
    auto set_cm = [&](float m) { asm volatile("" : "+v"(m));
#pragma unroll
        for (int i = 0; i < 16; i++) cm[i] = m; };
    auto half = [&](const f32x16& s, int hf, bool masked) {
        u32x4 p;
#pragma unroll
        for (int i = 0; i < 4; i++) { p[i] = pk_bf16(__builtin_amdgcn_exp2f(s[8 * hf + 2 * i]), __builtin_amdgcn_exp2f(s[8 * hf + 2 * i + 1])); if (masked) p[i] &= dmask[4 * hf + i]; }
        PIN4(p); return p; };
    f32x16 oA, oB, oC, oD, sa, sb;
    u32x4 p0, p1, pp[4][2];
#define TILE(S, MASK, P0, P1, M0, M1) do { P0 = half(S, 0, MASK); SB(); M0; SB(); P1 = half(S, 1, MASK); SB(); M1; SB(); } while (0)
    set_cm(mA); sb = mfp(kB, qB, cm); SB();
    sa = mfp(kA, qA, cm); SB();
    TILE(sb, true, p0, p1, (oB = mfp(vO[1][0], p0, zero)), (oB = mfp(vO[1][1], p1, oB)));
    STAMP(12);
    sb = mfp(kf[1][0], qA, cm); SB();
    STAMP(13);
    TILE(sa, true, p0, p1, (oA = mfp(vO[0][0], p0, zero)), (oA = mfp(vO[0][1], p1, oA)));
    {
        const u32 om = rg ? 0xffffffffu : 0u;
#pragma unroll
        for (int i = 0; i < 8; i++) dmask[i] = opaque(~dmask[i] | om);
    }
    SB();
    TILE(sb, false, pp[1][0], pp[1][1], (sa = mfp(kf[0][0], qA, cm)), (sb = mfp(kf[2][0], qA, cm)));
    TILE(sa, true, pp[0][0], pp[0][1], NOP_, (sa = mfp(kf[3][0], qA, cm)));
    asm volatile("" : "+v"(kf[0][1]), "+v"(kf[1][1]), "+v"(kf[2][1]), "+v"(kf[3][1]));
    STAMP(14);
    u32 vva[4];
#pragma unroll
    for (int t = 0; t < 4; t++) vva[t] = opaque(vab + (hso[t] & vam));
    lds_bits(flg + F_HALO * 4, vbits);
#pragma unroll
    for (int hd = 0; hd < 2; hd++)
#pragma unroll
        for (int t = 0; t < 4 - hd; t++) { vf[t][hd][0] = __builtin_amdgcn_raw_buffer_load_b128(bws, vva[t] + hd * 1024, 0, 16); vf[t][hd][1] = __builtin_amdgcn_raw_buffer_load_b128(bws, vva[t] + hd * 1024 + 512, 0, 16); }
    lds_sig(flg + F_VREQ * 4);
    SB();
    STAMP(15);
    TILE(sb, false, pp[2][0], pp[2][1], NOP_, (sb = mfp(kf[0][1], qB, cm)));
    TILE(sa, false, pp[3][0], pp[3][1], NOP_, (sa = mfp(kf[1][1], qB, cm)));
    TILE(sb, true, p0, p1, NOP_, (sb = mfp(kf[2][1], qB, cm)));
    STAMP(16);
    u32x4 p2, p3;
#define PVA(t, j) (oA = mfp(vf[t][0][j], pp[t][j], oA))
    PVA(0, 0); SB();
    STAMP(17);
    {
        auto quart = [&](const f32x16& s, int q, u32x4& p) { p[(2 * q) & 3] = pkp(__builtin_amdgcn_exp2f(s[4 * q]), __builtin_amdgcn_exp2f(s[4 * q + 1])); p[(2 * q + 1) & 3] = pkp(__builtin_amdgcn_exp2f(s[4 * q + 2]), __builtin_amdgcn_exp2f(s[4 * q + 3])); };
        quart(sa, 0, p2); SB(); PVA(0, 1); SB(); quart(sa, 1, p2); SB(); oB = mfp(vf[0][1][0], p0, oB); SB(); quart(sa, 2, p3); SB(); oB = mfp(vf[0][1][1], p1, oB); SB(); quart(sa, 3, p3); SB(); sa = mfp(kf[3][1], qB, cm); SB();
        quart(sb, 0, p0); SB(); PVA(1, 0); SB(); quart(sb, 1, p0); SB(); PVA(1, 1); SB();
        vf[3][1][0] = __builtin_amdgcn_raw_buffer_load_b128(bws, vva[3] + 1024, 0, 16); vf[3][1][1] = __builtin_amdgcn_raw_buffer_load_b128(bws, vva[3] + 1536, 0, 16); SB(); quart(sb, 2, p1); SB(); oB = mfp(vf[1][1][0], p2, oB); SB(); quart(sb, 3, p1); SB(); oB = mfp(vf[1][1][1], p3, oB); SB();
        asm volatile("" : "+v"(sa), "+v"(oA), "+v"(oB), "+v"(p0), "+v"(p1));
    }
    lds_cnt(flg + (F_Q + (cw ^ 1)) * 4, 1);
    const u32 ep = LB + L_EXQ + (cw ^ 1) * EXS;
    const u32x4 qC = lds_r128(ep + l16), qD = lds_r128(ep + 1024 + l16); const float mC = lds_rf(ep + 2048 + lane * 4);
    if (rg == 0) { kf[0][0] = lds_r128(own + l16); kf[0][1] = lds_r128(own + 1024 + l16); ld_ownv(vf[0]); }
    SB();
    {
        auto quart = [&](const f32x16& s, int q, u32x4& p) { p[(2 * q) & 3] = pkp(__builtin_amdgcn_exp2f(s[4 * q]), __builtin_amdgcn_exp2f(s[4 * q + 1])); p[(2 * q + 1) & 3] = pkp(__builtin_amdgcn_exp2f(s[4 * q + 2]), __builtin_amdgcn_exp2f(s[4 * q + 3])); };
        oB = mfp(vf[2][1][0], p0, oB); SB(); set_cm(mC); SB(); sb = mfp(kf[1][0], qC, cm); SB();
        quart(sa, 0, p2); SB(); oB = mfp(vf[2][1][1], p1, oB); SB(); quart(sa, 1, p2); SB(); PVA(2, 0); SB(); quart(sa, 2, p3); SB(); PVA(2, 1); SB(); quart(sa, 3, p3); SB(); sa = mfp(kf[2][0], qC, cm); SB();
        STAMP(18);
        p0 = half(sb, 0, true); SB(); oB = mfp(vf[3][1][0], p2, oB); SB(); PVA(3, 0); SB(); p1 = half(sb, 1, true); SB(); sb = mfp(kf[3][0], qC, cm); SB(); oB = mfp(vf[3][1][1], p3, oB); SB();
        quart(sa, 0, p2); SB(); PVA(3, 1); SB(); quart(sa, 1, p2); SB(); oC = mfp(vf[1][0][0], p0, zero); SB(); quart(sa, 2, p3); SB(); oC = mfp(vf[1][0][1], p1, oC); SB(); quart(sa, 3, p3); SB(); sa = mfp(kf[0][0], qC, cm); SB();
#define TILE3(S, MASK, PA, PB, M0, M1, M2) do { PA[0] = pkp(__builtin_amdgcn_exp2f(S[0]), __builtin_amdgcn_exp2f(S[1])); PA[1] = pkp(__builtin_amdgcn_exp2f(S[2]), __builtin_amdgcn_exp2f(S[3])); SB(); M0; SB(); \
            PA[2] = pkp(__builtin_amdgcn_exp2f(S[4]), __builtin_amdgcn_exp2f(S[5])); PA[3] = pkp(__builtin_amdgcn_exp2f(S[6]), __builtin_amdgcn_exp2f(S[7])); PB[0] = pkp(__builtin_amdgcn_exp2f(S[8]), __builtin_amdgcn_exp2f(S[9])); SB(); M1; SB(); \
            PB[1] = pkp(__builtin_amdgcn_exp2f(S[10]), __builtin_amdgcn_exp2f(S[11])); PB[2] = pkp(__builtin_amdgcn_exp2f(S[12]), __builtin_amdgcn_exp2f(S[13])); PB[3] = pkp(__builtin_amdgcn_exp2f(S[14]), __builtin_amdgcn_exp2f(S[15])); \
            if (MASK) { _Pragma("unroll") for (int i = 0; i < 4; i++) { PA[i] &= dmask[i]; PB[i] &= dmask[4 + i]; } } SB(); M2; SB(); } while (0)
        TILE3(sb, false, p0, p1, (oC = mfp(vf[2][0][0], p2, oC)), (oC = mfp(vf[2][0][1], p3, oC)), (sb = mfp(kf[1][1], qD, cm)));
        TILE3(sa, false, p2, p3, (oC = mfp(vf[3][0][0], p0, oC)), (oC = mfp(vf[3][0][1], p1, oC)), (sa = mfp(kf[2][1], qD, cm)));
        TILE3(sb, true, p0, p1, (oC = mfp(vf[0][0][0], p2, oC)), (oC = mfp(vf[0][0][1], p3, oC)), (sb = mfp(kf[3][1], qD, cm)));
        TILE3(sa, false, p2, p3, (oD = mfp(vf[1][1][0], p0, zero)), (oD = mfp(vf[1][1][1], p1, oD)), (sa = mfp(kf[0][1], qD, cm)));
        lds_wf4(ep + l16, Q4(oC, 0)); lds_wf4(ep + 1024 + l16, Q4(oC, 1)); SB();
        TILE3(sb, false, p0, p1, (oD = mfp(vf[2][1][0], p2, oD)), (oD = mfp(vf[2][1][1], p3, oD)), NOP_);
        p2 = half(sa, 0, false); SB(); oD = mfp(vf[3][1][0], p0, oD); SB(); oD = mfp(vf[3][1][1], p1, oD); SB();
        p3 = half(sa, 1, false); SB(); oD = mfp(vf[0][1][0], p2, oD); oD = mfp(vf[0][1][1], p3, oD);
    }
    lds_wf4(ep + 2048 + l16, Q4(oD, 0)); lds_wf4(ep + 3072 + l16, Q4(oD, 1)); *(LAS f32x2*)(ep + 4096 + lane * 8) = (f32x2){oC[8], oD[8]};
    lds_sig(flg + (F_O + cw) * 4);
    STAMP(19);
    ld_xe();
    lds_cnt(flg + (F_O + (cw ^ 1)) * 4, 1);
    STAMP(20);
    {
        const u32 e = LB + L_EXQ + cw * EXS;
        const f32x4 t0 = lds_rf4(e + l16), t1 = lds_rf4(e + 1024 + l16), t2 = lds_rf4(e + 2048 + l16), t3 = lds_rf4(e + 3072 + l16); const f32x2 tl = *(LAS f32x2*)(e + 4096 + lane * 8);
        float l[2] = {oA[8] + tl.x, oB[8] + tl.y};
        f32x4 o0[2] = {add4(Q4(oA, 0), t0), add4(Q4(oB, 0), t2)}, o1[2] = {add4(Q4(oA, 1), t1), add4(Q4(oB, 1), t3)};
        u32x4 op[2];
#pragma unroll
        for (int hd = 0; hd < 2; hd++) { const f32x4 inv = bc4(__builtin_amdgcn_rcpf(l[hd])); op[hd] = cat(scl4(o0[hd], inv), scl4(o1[hd], inv)); }
        u32 nf = 0;
#pragma unroll
        for (int i = 0; i < 8; i++) { const u32 v = op[i >> 2][i & 3]; nf |= ((v & 0x7f807f80u) + 0x00800080u); }
        if (__builtin_expect(FORCE_FB || !__all(l[0] >= 0x1p-60f && l[0] <= 0x1p60f && l[1] >= 0x1p-60f && l[1] <= 0x1p60f && (nf & 0x80008000u) == 0u), 0)) {
#pragma unroll 1
          for (int hd = 0; hd < 2; hd++) {
            const u32x4 qF = hd ? qB : qA;
            const int j0 = g >= 8 ? g - 8 : 0, head = pair * 2 + hd;
            { int gg = j0 + (lane & 15); gg = gg > g ? g : gg;
              const GAS u32* fp = (const GAS u32*)(ws + ((lane & 16) ? WS_FLAGV : WS_FLAGK) + (gg * 16 + pair) * FS);
              while (!__all(__hip_atomic_load(fp, RLX_AGENT) == tag) && ++spins <= SPIN_CAP) {}
              __builtin_amdgcn_fence(__ATOMIC_ACQUIRE, "wavefront"); }
            float mx = -3.0e38f;
            f32x16 o = zero;
#pragma unroll 1
            for (int pass = 0; pass < 2; pass++) {
#pragma unroll 1
                for (int j = j0; j <= g; j++) {
                    const u32 bo = (u32)(j * 32 + head) * 1024;
                    const f32x16 sc = mfma32(__builtin_amdgcn_raw_buffer_load_b128(bws, l16, WS_K + bo, 16), qF, zero);
                    const int dq = (g - j) * 32 + l31 - 4 * h;
                    float p[16];
#pragma unroll
                    for (int i = 0; i < 16; i++) { const int d = dq - (i & 3) - 8 * (i >> 2); const bool vis = d >= 0 && d < 256;
                        if (vis) mx = pass ? mx : fmaxf(mx, sc[i]); p[i] = vis ? __builtin_amdgcn_exp2f(sc[i] - mx) : 0.f; }
                    if (pass) {
                        const u32 va = (lane & 16) ? WS_ONES + b * 2048 : WS_V + vlo + bo;
                        o = mfma32(__builtin_amdgcn_raw_buffer_load_b128(bws, va, 0, 16), (u32x4){pk_bf16(p[0], p[1]), pk_bf16(p[2], p[3]), pk_bf16(p[4], p[5]), pk_bf16(p[6], p[7])}, o);
                        o = mfma32(__builtin_amdgcn_raw_buffer_load_b128(bws, va + 512, 0, 16), (u32x4){pk_bf16(p[8], p[9]), pk_bf16(p[10], p[11]), pk_bf16(p[12], p[13]), pk_bf16(p[14], p[15])}, o);
                    }
                }
                auto sw = __builtin_amdgcn_permlane32_swap(__float_as_uint(mx), __float_as_uint(mx), false, false);
                mx = fmaxf(__uint_as_float(sw[0]), __uint_as_float(sw[1]));
            }
            const f32x4 inv = bc4(__builtin_amdgcn_rcpf(o[8]));
            const u32x4 r = cat(scl4(Q4(o, 0), inv), scl4(Q4(o, 1), inv));
            if (hd) op[1] = r; else op[0] = r;
          }
        }
        st_sc1(bws, WS_O + blk + l16, op[0]); st_sc1(bws, WS_O + 1024 + blk + l16, op[1]);
    }
    STAMP(21);
    SB();
    lds_cnt(flg + F_WO * 4, 2);
    u32x4 wof[32];
    {
        const int q = (lane & 15) >> 2;
        const u32 a = LB + (u32)(4 * h + q) * 256 + ((cw ^ q) << 6) + (((lane >> 4) & 1) << 5) + ((lane & 3) << 3), a2 = opaque(a + GR);
#pragma unroll
        for (int s = 0; s < 32; s++) { const u32 c = (s < 16 ? a : a2) + (s & 15) * 4096; wof[s] = cat(tr_read(c), tr_read(c + 2048)); }
        lds_sig(flg + F_WOF * 4);
    }
    SB();
    WAITV(0); st_flag(WS_FLAGO + fo);
    STAMP(22);
    SB();
    calc_g();
    STAMP(25);
    {
        u32x4 of[2][8];
        auto rd_o = [&](int qd) { lds_cnt(flg + (F_OST + qd) * 4, 1);
#pragma unroll
            for (int s = 0; s < 8; s++) of[qd & 1][s] = lds_r128(LB + L_OST + (qd * 8 + s) * 1024 + l16); };
        rd_o(0);
        STAMP(28);
#pragma unroll
        for (int qd = 0; qd < 4; qd++) {
            SB();
            acc = mfp(wof[qd * 8], of[qd & 1][0], qd ? acc : zero); acc = mfp(wof[qd * 8 + 1], of[qd & 1][1], acc); SB();
            if (qd < 3) rd_o(qd + 1);
            SB();
#pragma unroll
            for (int s = 2; s < 8; s++) acc = mfp(wof[qd * 8 + s], of[qd & 1][s], acc);
        }
        const u32 a = LB + L_TR + l31 * TP + cw * 128 + h * 16;
#pragma unroll
        for (int q = 0; q < 4; q++) lds_wf4(a + q * 32, Q4(acc, q));
    }
    } else if (wv == 2) {
                __builtin_amdgcn_s_setprio(3);
                const u32x4 o4 = {ONES, ONES, ONES, ONES};
                *(LAS u32*)(LB + L_FLG + lane * 4) = 0u;
                const u32 ep_v = __hip_atomic_load((const GAS u32*)(ws + WS_EPOCH), RLX_AGENT);
                lds_w128(LB + L_ONES + l16, o4); lds_w128(LB + L_ONES + 1024 + l16, o4);
                st_sc1(bws, WS_ONES + b * 2048 + l16, o4); st_sc1(bws, WS_ONES + b * 2048 + 1024 + l16, o4);
                ld_xe();
                BAR();
                *(volatile LAS u32*)(flg + F_TAG * 4) = ep_v + 1u;
                tag = __builtin_amdgcn_readfirstlane(ep_v) + 1u;
                BAR();
                RBAR();
                {
                    const u32 sw = ((lane & 7) ^ (((lane >> 4) & 1) << 2)) << 4, nb = b < 224 ? 512 * 1792 * 2 : 0;
#pragma unroll
                    for (int mt = 0; mt < 3; mt++) {
                        RBAR();
                        const i32x4 rf = mk_rsrc(mt == 0 ? wg : mt == 1 ? wu : wd, nb);
                        const u32 rp = mt < 2 ? 3584 : 1024;
                        u32 so = mt < 2 ? (u32)(b & 7) * 64 * 3584 + (u32)(b >> 3) * 128 : (u32)(b >> 3) * 65536 + (u32)(b & 7) * 128, ld = LB + L_FW + mt * SL;
#pragma unroll
                        for (int j = 0; j < 8; j++) { dma16(ld, (u32)(lane >> 3) * rp + sw, rf, so); ld += 1024; so += 8 * rp; }
                    }
                }
                WAITV(0);
                STAMP(4);
                u32x4 fw[3][4][2];
                auto rd_fw = [&]() {
                    const u32 wa = LB + L_FW + (u32)(8 * (lane >> 4) + ((lane & 15) >> 2)) * 128 + ((lane & 3) << 3), sw = ((lane >> 3) & 1) << 6;
#pragma unroll
                    for (int ct = 0; ct < 4; ct++) { const u32 a = opaque(wa + ((((ct >> 1) << 6) ^ sw) + (ct & 1) * 32));
#pragma unroll
                        for (int mt = 0; mt < 3; mt++) { fw[mt][ct][0] = cat(tr_read(a + mt * SL), tr_read(a + mt * SL + 512)); fw[mt][ct][1] = cat(tr_read(a + mt * SL + 4096), tr_read(a + mt * SL + 4096 + 512)); } }
                    lds_sig(flg + F_MW * 4); };
                rd_fw();
                if (b < 224) {
                    lds_cnt(flg + F_SEL * 4, 1);
                    const u32 sel = *(volatile LAS u32*)(LB + L_SEL + (lane & 7) * 4);
                    const int srow = 2047 - (int)(sel & 2047u);
                    const u16* xp = x + (size_t)srow * 512 + (b & 7) * 64 + (lane >> 4) * 8;
                    u32x2 st = __builtin_amdgcn_raw_buffer_load_b64(bws, WS_S + srow * 8, 0, 16);
                    u32x4 xs0 = *(const GAS u32x4*)xp, xs1 = *(const GAS u32x4*)(xp + 32);
                    asm volatile("" : "+v"(xs0), "+v"(xs1) :: "memory");
                    while (!__all(st.y == tag) && ++spins <= SPIN_CAP) { st = __builtin_amdgcn_raw_buffer_load_b64(bws, WS_S + srow * 8, 0, 16); asm volatile("" ::: "memory"); }
                    STAMP(10);
                    const float m = bf_lo(st.x), rs = __builtin_amdgcn_rcpf(bf_hi(st.x));
#pragma unroll
                    for (int e = 0; e < 4; e++) { xs0[e] = norm2(xs0[e], m, rs); xs1[e] = norm2(xs1[e], m, rs); }
                    const u32 go = WS_GU + (u32)b * 4096 + ((lane >> 4) * 32 + (lane & 8) * 2 + (lane & 7)) * 8;
                    const u32 hi8 = (lane & 8) ? 0xffffffffu : 0u;
#pragma unroll
                    for (int ct = 0; ct < 4; ct++) {
                        const f32x4 d0 = mfma16(fw[0][ct][1], xs1, mfma16(fw[0][ct][0], xs0, bc4(0.f))), d1 = mfma16(fw[1][ct][1], xs1, mfma16(fw[1][ct][0], xs0, bc4(0.f)));
#pragma unroll
                        for (int e = 0; e < 2; e++)
                            __builtin_amdgcn_raw_buffer_store_b64((u32x2){(__float_as_uint(d0[2 + e]) & hi8) | (__float_as_uint(d0[e]) & ~hi8), (__float_as_uint(d1[2 + e]) & hi8) | (__float_as_uint(d1[e]) & ~hi8)}, bws, go + (ct * 16 + e) * 64, 0, 16);
                    }
                    WAITV(0); st_flag(WS_FGU + b * FS);
                    STAMP(9);
                    lds_cnt(flg + F_VREQ * 4, 4);
                    ffn_h();
                    lds_cnt(flg + F_H * 4, 2);
                    const u32x4 h0 = lds_r128(LB + L_H + (lane & 7) * 128 + (lane >> 4) * 16), h1 = lds_r128(LB + L_H + (lane & 7) * 128 + 64 + (lane >> 4) * 16);
                    const u32 yo = WS_Y + (u32)(((lane & 7) * 8 + (b & 7)) * 28 + (b >> 3)) * 512 + (lane >> 4) * 32 + (lane & 8) * 2;
#pragma unroll
                    for (int ct = 0; ct < 4; ct++) {
                        const f32x4 d = mfma16(fw[2][ct][1], h1, mfma16(fw[2][ct][0], h0, bc4(0.f)));
                        st_sc1(bws, yo + ct * 128, (u32x4){(__float_as_uint(d.z) & hi8) | (__float_as_uint(d.x) & ~hi8), tag, (__float_as_uint(d.w) & hi8) | (__float_as_uint(d.y) & ~hi8), tag});
                    }
                    STAMP(12);
                }
                gather_o();
    } else {
                __builtin_amdgcn_s_setprio(3);
                const int rrow = rb * 64 + hq * 8 + (lane >> 3);
                u32x4 xr[8], wr_[8];
#pragma unroll
                for (int i = 0; i < 8; i++) { xr[i] = *(const GAS u32x4*)(x + (size_t)rrow * 512 + i * 64 + (lane & 7) * 8); wr_[i] = *(const GAS u32x4*)(wr + i * 64 + (lane & 7) * 8); }
                BAR();
                float a0 = 0, a1 = 0, a2 = 0, a3 = 0;
#pragma unroll
                for (int i = 0; i < 8; i++) {
#pragma unroll
                    for (int e = 0; e < 4; e++) { a0 = dot2(xr[i][e], ONES, a0); a1 = dot2(xr[i][e], xr[i][e], a1); a2 = dot2(xr[i][e], wr_[i][e], a2); a3 = dot2(wr_[i][e], ONES, a3); }
                    if (i == 3) { asm volatile("" : "+v"(a0), "+v"(a1), "+v"(a2), "+v"(a3)); RBAR(); }
                }
                asm volatile("" : "+v"(a0), "+v"(a1), "+v"(a2), "+v"(a3));
                RBAR();
                tag = __builtin_amdgcn_readfirstlane(*(volatile LAS u32*)(flg + F_TAG * 4));
                {
                    const float mean = red8(a0) * (1.0f / 512.0f), var = fmaxf(red8(a1) * (1.0f / 512.0f) - mean * mean, 0.f);
                    const float m = rbf(mean), sd = rbf(__builtin_amdgcn_sqrtf(rbf(rbf(var) + 1e-5f)));
                    const u32 rbits = pk_bf16((red8(a2) - m * red8(a3)) * __builtin_amdgcn_rcpf(sd), 0.f) & 0xffffu;
                    const u32 key = (((rbits & 0x8000u) ? (~rbits & 0xffffu) : (rbits | 0x8000u)) << 11) | (u32)(2047 - rrow);
                    if ((lane & 7) < 2) __builtin_amdgcn_raw_buffer_store_b64((u32x2){(lane & 1) ? pk_bf16(m, sd) : key, tag}, bws, ((lane & 1) ? WS_S : WS_R) + rrow * 8, 0, 16);
                }
                RBAR(); RBAR(); RBAR();
                u32x4 kq[16];
                auto gather = [&]() {
#pragma unroll
                    for (int i = 0; i < 16; i++) kq[i] = __builtin_amdgcn_raw_buffer_load_b128(bws, l16 + i * 1024, WS_R, 16);
                    asm volatile("" ::: "memory"); };
                auto stale = [&]() { u32 d = 0;
#pragma unroll
                    for (int i = 0; i < 16; i++) d |= (kq[i].y ^ tag) | (kq[i].w ^ tag);
                    return !__all(d == 0u); };
                gather();
                while (stale() && ++spins <= SPIN_CAP) gather();
                STAMP(7);
                u32 m1 = 0, m2 = 0, m3 = 0;
#pragma unroll
                for (int i = 0; i < 32; i++) { const u32 k = kq[i >> 1][(i & 1) * 2];
                    asm("v_med3_u32 %0, %1, %0, %2" : "+v"(m3) : "v"(m2), "v"(k)); asm("v_med3_u32 %0, %1, %0, %2" : "+v"(m2) : "v"(m1), "v"(k)); m1 = m1 > k ? m1 : k; }
                u32 thr = 0;
#pragma unroll 1
                for (u32 bit = 1u << 26; bit; bit >>= 1) {
                    const u32 t = thr | bit;
                    const int c = __builtin_popcountll(__ballot(m1 >= t)) + __builtin_popcountll(__ballot(m2 >= t));
                    if (c >= 8) { thr = t; if (c == 8) break; }
                }
                if (__builtin_expect(FORCE_SEL || __any(m3 >= thr), 0)) {
                    thr = 0;
#pragma unroll 1
                    for (u32 bit = 1u << 26; bit; bit >>= 1) {
                        const u32 t = thr | bit; int c = 0;
#pragma unroll
                        for (int i = 0; i < 32; i++) c += __builtin_popcountll(__ballot(kq[i >> 1][(i & 1) * 2] >= t));
                        if (c >= 8) thr = t;
                    }
                    int pos = 0;
#pragma unroll
                    for (int i = 0; i < 32; i++) { const u32 k = kq[i >> 1][(i & 1) * 2]; const u64 bm = __ballot(k >= thr);
                        const int p = pos + __builtin_amdgcn_mbcnt_hi((u32)(bm >> 32), __builtin_amdgcn_mbcnt_lo((u32)bm, 0u));
                        if (k >= thr) *(LAS u32*)(LB + L_SEL + (p & 7) * 4) = k; pos += __builtin_popcountll(bm); }
                } else {
                    const u64 b1 = __ballot(m1 >= thr), b2 = __ballot(m2 >= thr);
                    const int p1 = __builtin_amdgcn_mbcnt_hi((u32)(b1 >> 32), __builtin_amdgcn_mbcnt_lo((u32)b1, 0u));
                    const int p2 = __builtin_popcountll(b1) + __builtin_amdgcn_mbcnt_hi((u32)(b2 >> 32), __builtin_amdgcn_mbcnt_lo((u32)b2, 0u));
                    if (m1 >= thr) *(LAS u32*)(LB + L_SEL + (p1 & 7) * 4) = m1;
                    if (m2 >= thr) *(LAS u32*)(LB + L_SEL + (p2 & 7) * 4) = m2;
                }
                asm volatile("" ::: "memory");
                lds_sig(flg + F_SEL * 4);
                STAMP(8);
                ld_xe();
                lds_cnt(flg + F_VREQ * 4, 4);
                ffn_h();
                if (b == 0) {
                    for (;;) { bool ok = true;
#pragma unroll
                        for (int i = 0; i < 4; i++) { const int w = lane + 64 * i; ok &= __hip_atomic_load((const GAS u32*)(ws + WS_FLAGK + ((w >> 3) * 2 * 16 + (w & 7) * 2) * FS), RLX_AGENT) == tag; }
                        if (__all(ok) || ++spins > SPIN_CAP) break;
                    }
                    st_flag(WS_EPOCH);
                }
                gather_o();
    }

    STAMP(29);
    SB(); BAR();
    STAMP(30);
    {
        const u32 a = LB + L_TR + (tid >> 4) * TP + (tid & 15) * 32;
        const u32x4 sp4 = pack8(lds_rf4(a), lds_rf4(a + 16));
        const u32 selk = *(LAS u32*)(LB + L_SEL + (lane & 7) * 4);
        for (u32 mm = (u32)__ballot(((2047u - (selk & 2047u)) >> 2) == (u32)(og * 8 + wv)) & 0xffu; mm; mm &= mm - 1) {
            const int tk = __builtin_ctz(mm);
            const u32 key = __builtin_amdgcn_readlane(selk, tk);
            if (2047 - (int)(key & 2047u) == orow) {
                const u32 k16 = key >> 11, rbits = (k16 & 0x8000u) ? (k16 & 0x7fffu) : (~k16 & 0xffffu);
                const float rsel = rbf(__builtin_amdgcn_rcpf(1.0f + __builtin_amdgcn_exp2f(bf_lo(rbits) * -1.4426950408889634f)));
                const u32 ya = LB + L_YR + tk * 2048 + (tid & 15) * 32;
                const u32x4 yp = pack8((lds_rf4(ya) + lds_rf4(ya + 512)) + (lds_rf4(ya + 1024) + lds_rf4(ya + 1536)), (lds_rf4(ya + 16) + lds_rf4(ya + 528)) + (lds_rf4(ya + 1040) + lds_rf4(ya + 1552)));
#pragma unroll
                for (int e = 0; e < 4; e++) {
                    const u32 yy = pk_bf16(bf_lo(yp[e]) * rsel, bf_hi(yp[e]) * rsel);
                    const u32 xb = pk_bf16(bf_lo(xnp[e]) + bf_lo(yy), bf_hi(xnp[e]) + bf_hi(yy));
                    gp[e] = pk_bf16(silu_(bf_lo(xb)), silu_(bf_hi(xb)));
                }
            }
        }
        u32x4 ov;
#pragma unroll
        for (int e = 0; e < 4; e++) {
            const u32 t = pk_bf16(bf_lo(sp4[e]) * bf_lo(gp[e]), bf_hi(sp4[e]) * bf_hi(gp[e]));
            ov[e] = pk_bf16(bf_lo(xe[e]) + bf_lo(t), bf_hi(xe[e]) + bf_hi(t));
        }
        __builtin_amdgcn_raw_buffer_store_b128(ov, __builtin_amdgcn_make_buffer_rsrc(out, 0, 2048 * 512 * 2, 0x00020000), (u32)(orow * 512 + ocol) * 2, 0, OUTAUX);
    }
}
#define STAMP_WAVES 8
#define STAMP_NAMES {"start","pre/L:x0 in","P0 ok/L:Wk in","L:Wv in","L:Wq in","K mfma/L:Vreq/P:halo","K stored/L:Wo in","flagK/R:gath","V stored/R:sel","flagV/R:A1","K issued/R:xs","Q done/R:GUok","diagB/R:A2","K landed","P01","V issued","P23","PV A","F done","S done","O ok","o stored","flagO","","P:y done","g done","L:o poll ok","L:o in/P:y","o q0 ok","arr Bs","rel Bs"}

extern "C" void kernel_launch(void* const* d_in, const int* in_sizes, int n_in, void* d_out, int out_size, void* d_ws, size_t ws_size, hipStream_t stream) {
    fused_kernel<<<256, 512, 0, stream>>>((const u16*)d_in[0], (const u16*)d_in[1], (char*)d_ws, (const u16*)d_in[2], (u16*)d_out, (const u16*)d_in[4], (const u16*)d_in[5], (const u16*)d_in[6], (const u16*)d_in[7]);
}
```

```cpp
#include <hip/hip_runtime.h>
#include <stdint.h>

typedef unsigned short u16;
typedef unsigned u32;
typedef unsigned long long u64;
typedef u32 u32x4 __attribute__((ext_vector_type(4)));
typedef u32 u32x2 __attribute__((ext_vector_type(2)));
typedef int i32x4 __attribute__((ext_vector_type(4)));
typedef __bf16 bf16x8 __attribute__((ext_vector_type(8)));
typedef __bf16 bf16x4 __attribute__((ext_vector_type(4)));
typedef __bf16 bf16x2 __attribute__((ext_vector_type(2)));
typedef float f32x16 __attribute__((ext_vector_type(16)));
typedef float f32x4 __attribute__((ext_vector_type(4)));
typedef float f32x2 __attribute__((ext_vector_type(2)));
#define LAS __attribute__((address_space(3)))
#define GAS __attribute__((address_space(1)))
#define DI __device__ __forceinline__
#define RLX_AGENT __ATOMIC_RELAXED, __HIP_MEMORY_SCOPE_AGENT

constexpr u32 WS_EPOCH = 0;
constexpr u32 FS = 64;
constexpr u32 WS_FLAGK = 128 << 10;
constexpr u32 WS_FLAGV = 256 << 10;
constexpr u32 WS_FLAGO = 384 << 10;
constexpr u32 WS_ONES = 512 << 10;
constexpr u32 WS_R = 64 << 10;
constexpr u32 WS_S = 80 << 10;
constexpr u32 WS_FGU = 96 << 10;
constexpr u32 WS_GU = 7 << 20;
constexpr u32 WS_Y = 8 << 20;
constexpr u32 WS_K = 1 << 20;
constexpr u32 WS_V = 3 << 20;
constexpr u32 WS_O = 5 << 20;
constexpr u32 WS_USED = 9 << 20;
#define STAMP(i) do {} while (0)

#define STAMPS_STEP(n) do {} while (0)
constexpr u32 SL = 8192, GR = 8 * SL;
constexpr u32 L_SCR = 2 * GR;
constexpr u32 L_EXQ = L_SCR;
constexpr u32 EXS = 4608;
constexpr u32 L_OWN = L_SCR + 18432;
constexpr u32 L_STP = L_SCR + 26624;
constexpr u32 L_H = L_STP;
constexpr u32 L_CS = L_SCR + 27648;
constexpr u32 L_ST = L_SCR + 29184;
constexpr u32 L_FLG = L_SCR + 29696;
constexpr u32 L_SEL = L_SCR + 29952;
constexpr u32 L_DEAD = L_SCR + 30208;
constexpr u32 L_ONES = L_SCR + 30720;
constexpr u32 L_FW = L_SCR;
constexpr u32 L_OST = 65536;
constexpr u32 L_TR = 0;
constexpr u32 L_YR = 98304;
constexpr u32 L_TOTAL = 163840;
__device__ constexpr u32 fill_slot(int f) { return f < 4 ? 4 + f : ((f - 4) >> 1) + ((f & 1) ? 4 : 0); }
constexpr u32 F_CS = 0;
constexpr u32 F_CSV = 1;
constexpr u32 F_LAND = 21;
constexpr u32 F_FW = 24;
constexpr u32 F_HALO = 2;
constexpr u32 F_MW = 3;
constexpr u32 F_VREQ = 4;
constexpr u32 F_WO = 5;
constexpr u32 F_WOF = 6;
constexpr u32 F_SEL = 7;
constexpr u32 F_Q = 8;
constexpr u32 F_O = 12;
constexpr u32 F_OST = 16;
constexpr u32 F_H = 20;
constexpr u32 F_TAG = 25;

DI float bf_lo(u32 v) { return __uint_as_float(v << 16); }
DI float bf_hi(u32 v) { return __uint_as_float(v & 0xffff0000u); }
DI u32 pk_bf16(float a, float b) { f32x2 f = {a, b}; return __builtin_bit_cast(u32, __builtin_convertvector(f, bf16x2)); }
DI float rbf(float a) { return bf_lo(pk_bf16(a, a)); }
DI float dot2(u32 a, u32 b, float c) { return __builtin_amdgcn_fdot2_f32_bf16(__builtin_bit_cast(bf16x2, a), __builtin_bit_cast(bf16x2, b), c, false); }
DI f32x16 mfma32(u32x4 a, u32x4 b, f32x16 c) { return __builtin_amdgcn_mfma_f32_32x32x16_bf16(__builtin_bit_cast(bf16x8, a), __builtin_bit_cast(bf16x8, b), c, 0, 0, 0); }
DI f32x4 mfma16(u32x4 a, u32x4 b, f32x4 c) { return __builtin_amdgcn_mfma_f32_16x16x32_bf16(__builtin_bit_cast(bf16x8, a), __builtin_bit_cast(bf16x8, b), c, 0, 0, 0); }
DI u32x2 tr_read(u32 addr) { return __builtin_bit_cast(u32x2, __builtin_amdgcn_ds_read_tr16_b64_v4bf16((LAS bf16x4*)addr)); }
DI u32x4 lds_r128(u32 addr) { return *(LAS u32x4*)addr; }
DI f32x4 lds_rf4(u32 addr) { return *(LAS f32x4*)addr; }
DI void lds_w128(u32 addr, u32x4 v) { *(LAS u32x4*)addr = v; }
DI void lds_wf4(u32 addr, f32x4 v) { *(LAS f32x4*)addr = v; }
template <int CTRL> DI float dpp_add(float v) { return v + __uint_as_float(__builtin_amdgcn_update_dpp(0u, __float_as_uint(v), CTRL, 0xf, 0xf, true)); }
DI float red8(float v) { v = dpp_add<0xB1>(v); v = dpp_add<0x4E>(v); return dpp_add<0x141>(v); }
DI i32x4 mk_rsrc(const void* p, u32 bytes) { u64 a = (u64)p; i32x4 r; r.x = (int)(u32)a; r.y = (int)(u32)(a >> 32) & 0xffff; r.z = (int)bytes; r.w = 0x00020000; return r; }
DI void dma16(u32 lds, u32 voff, i32x4 rsrc, u32 soff) {
    asm volatile("s_mov_b32 m0, %0\n\ts_nop 0\n\tbuffer_load_dwordx4 %1, %2, %3 offen lds" :: "s"(lds), "v"(voff), "s"(rsrc), "s"(soff) : "memory");
}
DI void touch(u32 lds, const GAS void* p) { asm volatile("s_mov_b32 m0, %0\n\ts_nop 0\n\tglobal_load_lds_dword %1, off" :: "s"(lds), "v"(p) : "memory"); }
DI void dma16c(u32 lds, u32 voff, i32x4 rsrc, u32 soff) {
    asm volatile("s_mov_b32 m0, %0\n\ts_nop 0\n\tbuffer_load_dwordx4 %1, %2, %3 offen sc1 lds" :: "s"(lds), "v"(voff), "s"(rsrc), "s"(soff) : "memory");
}
DI u32 opaque(u32 v) { asm volatile("" : "+v"(v)); return v; }
DI u32x4 pack8(f32x4 a, f32x4 b) { return (u32x4){pk_bf16(a.x, a.y), pk_bf16(a.z, a.w), pk_bf16(b.x, b.y), pk_bf16(b.z, b.w)}; }
#define Q4(acc, q) ((f32x4){acc[4 * (q)], acc[4 * (q) + 1], acc[4 * (q) + 2], acc[4 * (q) + 3]})
#define BAR() asm volatile("s_waitcnt lgkmcnt(0)\n\ts_barrier" ::: "memory")
#define RBAR() asm volatile("s_barrier" ::: "memory")
#define WAITV_(n) asm volatile("s_waitcnt vmcnt(" #n ")" ::: "memory")
#define WAITV(n) ({ WAITV_(n); })
constexpr u32 SPIN_CAP = 1u << 20;
#define XDELAY 6
#define WOEARLY 0
#define FORCE_FB 0
#define WARM 0
#define CODE_LO 0
#define CODE_N 3
#define OUTAUX 16
#define WO1 2
#define FORCE_SEL 0
#define CPF 1
#define ROTX 2
#define WOQ 4
constexpr u32 ONES = 0x3f803f80u;

#define SB() __builtin_amdgcn_sched_barrier(0)
DI float np(float r) { asm("" : "+v"(r)); return r; }
DI u32x2 scl4(f32x4 a, f32x4 r) { return (u32x2){pk_bf16(np(a.x * r.x), np(a.y * r.y)), pk_bf16(np(a.z * r.z), np(a.w * r.w))}; }
DI u32x4 split3(float c) {
    const u32 a = pk_bf16(c, c); const float r1 = np(c - bf_lo(a));
    const u32 b = pk_bf16(r1, r1); const float r2 = np(r1 - bf_lo(b));
    return (u32x4){(a & 0xffffu) | (b & 0xffff0000u), pk_bf16(r2, 0.f), 0u, 0u};
}
DI f32x4 addmul4(f32x4 a, f32x4 b, float m) { return (f32x4){np(np(a.x + b.x) * m), np(np(a.y + b.y) * m), np(np(a.z + b.z) * m), np(np(a.w + b.w) * m)}; }
DI f32x4 add4(f32x4 a, f32x4 b) { return (f32x4){np(a.x + b.x), np(a.y + b.y), np(a.z + b.z), np(a.w + b.w)}; }
DI f32x4 bc4(float a) { return (f32x4){a, a, a, a}; }
DI u32x4 cat(u32x2 a, u32x2 b) { return (u32x4){a.x, a.y, b.x, b.y}; }
#define PIN4(v) asm volatile("" : "+v"(v))
DI void lds_sig_(u32& pt, u32 addr) {
    asm volatile("v_mov_b32 %0, %1\n\ts_mov_b64 exec, 1\n\tds_add_u32 %0, %0\n\ts_mov_b64 exec, -1" : "+v"(pt) : "s"(addr) : "memory");
}
DI void lds_need_(u32& pt, u32& have, u32 addr, u32 n) {
    u32 c;
    asm volatile("s_cmp_ge_u32 %0, %4\n\ts_cbranch_scc1 2f\n\ts_mov_b32 %2, 0x400000\n"
                 "1:\n\tv_mov_b32 %1, %3\n\tds_read_b32 %1, %1\n\ts_waitcnt lgkmcnt(0)\n\tv_readfirstlane_b32 %0, %1\n\ts_cmp_ge_u32 %0, %4\n\ts_cbranch_scc1 2f\n\ts_sub_u32 %2, %2, 1\n\ts_cbranch_scc0 1b\n2:"
                 : "+s"(have), "+v"(pt), "=&s"(c) : "s"(addr), "s"(n) : "memory", "scc");
}
DI void lds_cnt_(u32& pt, u32 addr, u32 n) {
    u32 c, v;
    asm volatile("s_mov_b32 %2, 0x400000\n"
                 "1:\n\tv_mov_b32 %1, %3\n\tds_read_b32 %1, %1\n\ts_waitcnt lgkmcnt(0)\n\tv_readfirstlane_b32 %0, %1\n\ts_cmp_ge_u32 %0, %4\n\ts_cbranch_scc1 2f\n\ts_sub_u32 %2, %2, 1\n\ts_cbranch_scc0 1b\n2:"
                 : "=&s"(v), "+v"(pt), "=&s"(c) : "s"(addr), "s"(n * addr) : "memory", "scc");
}
DI void lds_bits_(u32& pt, u32 addr, u32 bits) {
    u32 c, v;
    asm volatile("s_mov_b32 %2, 0x400000\n"
                 "1:\n\tv_mov_b32 %1, %3\n\tds_read_b32 %1, %1\n\ts_waitcnt lgkmcnt(0)\n\tv_readfirstlane_b32 %0, %1\n\ts_andn2_b32 %0, %4, %0\n\ts_cbranch_scc0 2f\n\ts_sub_u32 %2, %2, 1\n\ts_cbranch_scc0 1b\n2:"
                 : "=&s"(v), "+v"(pt), "=&s"(c) : "s"(addr), "s"(bits) : "memory", "scc");
}
#define lds_sig(a) lds_sig_(pt, a)
#define lds_need(h, a, n) lds_need_(pt, h, a, n)
#define lds_cnt(a, n) lds_cnt_(pt, a, n)
#define lds_bits(a, b) lds_bits_(pt, a, b)
DI f32x16 mfp(u32x4 a, u32x4 b, f32x16 c) { f32x16 r = mfma32(a, b, c); PIN4(r); return r; }
DI u32 pkp(float a, float b) { return opaque(pk_bf16(a, b)); }
template <int CTRL, int RM> DI u32 dpp_umax(u32 v) { const u32 o = __builtin_amdgcn_update_dpp(0u, v, CTRL, RM, 0xf, false); return v > o ? v : o; }
DI u32 wave_umax(u32 v) { v = dpp_umax<0x111, 0xf>(v); v = dpp_umax<0x112, 0xf>(v); v = dpp_umax<0x114, 0xf>(v); v = dpp_umax<0x118, 0xf>(v); v = dpp_umax<0x142, 0xa>(v); v = dpp_umax<0x143, 0xc>(v);
    return __builtin_amdgcn_readlane(v, 63); }
DI float silu_(float x) { return x * __builtin_amdgcn_rcpf(1.0f + __builtin_amdgcn_exp2f(x * -1.4426950408889634f)); }
DI u32 norm2(u32 x, float m, float rs) { const u32 d = pk_bf16(bf_lo(x) - m, bf_hi(x) - m); return pk_bf16(bf_lo(d) * rs, bf_hi(d) * rs); }
DI void st_sc1(__amdgpu_buffer_rsrc_t r, u32 off, u32x4 v) { __builtin_amdgcn_raw_buffer_store_b128(v, r, off, 0, 16); }
DI float swap_add(float m) { auto sw = __builtin_amdgcn_permlane32_swap(__float_as_uint(m), __float_as_uint(m), false, false); return __uint_as_float(sw[0]) + __uint_as_float(sw[1]); }
DI float lds_rf(u32 a) { return *(LAS float*)a; }
DI void lds_wf(u32 a, float v) { *(LAS float*)a = v; }

__global__ __launch_bounds__(512, 2) void fused_kernel(const u16* __restrict__ x, const u16* __restrict__ wqkv, char* __restrict__ ws, const u16* __restrict__ wo, u16* __restrict__ out,
                                                        const u16* __restrict__ wr, const u16* __restrict__ wg, const u16* __restrict__ wu, const u16* __restrict__ wd) {
    asm volatile("" :: "s"(x), "s"(wqkv), "s"(ws), "s"(wo), "s"(out), "s"(wr), "s"(wg), "s"(wu), "s"(wd));
    __shared__ __attribute__((aligned(1024))) char lds_mem[L_TOTAL];
    const u32 LB = (u32)(uintptr_t)lds_mem;
    int b = blockIdx.x, tid = threadIdx.x;
    const int wv = __builtin_amdgcn_readfirstlane(tid >> 6);
    if (wv < 2) {
        const int rb = ((b & 7) >> 1) * 8 + (b >> 5), hq = (b & 1) * 4 + ((b >> 3) & 3), rot = (rb + ROTX * hq) & 3, lane = tid & 63;
        const i32x4 rx = mk_rsrc(x, 2048 * 512 * 2), rw = mk_rsrc(wqkv, 512 * 1536 * 2);
        const u32 vx0 = (u32)(lane >> 3) * 1024 + (((lane & 7) ^ ((lane >> 4) & 3)) << 4), vx1 = vx0 ^ 64;
        u32 so = (u32)(rb * 64) * 1024 + (u32)rot * 256 + wv * 128, ld = LB + wv * GR;
#pragma unroll
        for (int j = 0; j < 8; j++) { dma16(ld, (j & 1) ? vx1 : vx0, rx, so); ld += 1024; so += 8 * 1024; }
        const u32 vw = (u32)(lane >> 3) * 3072 + (((lane & 7) ^ (((lane >> 4) & 1) << 2)) << 4);
        so = ((u32)rot * 128 + wv * 64) * 3072 + 1024 + hq * 128; ld = LB + wv * GR + 4 * SL;
#pragma unroll
        for (int j = 0; j < 8; j++) { dma16(ld, vw, rw, so); ld += 1024; so += 8 * 3072; }
    }
    else if (wv >= 4 || wv == 2) {
        u64 pc; asm volatile("s_getpc_b64 %0" : "=s"(pc));
        const int piece = wv == 2 ? 4 : wv - 4;
        if (((b >> 3) & 3) == (piece & 3)) touch(LB + L_DEAD, (const GAS void*)((pc & ~(u64)1023) + (piece * 64 + (tid & 63)) * 128));
    }
    asm volatile("" : "+s"(b), "+v"(tid));
    const int xcd = b & 7, bi = b >> 3;
    const int rb = (xcd >> 1) * 8 + (bi >> 2), hq = (xcd & 1) * 4 + (bi & 3);
    const int lane = tid & 63;
    const int rot = (rb + ROTX * hq) & 3;
    const int h = lane >> 5, l31 = lane & 31;
#define STAMP_OUT() do {} while (0)

    const u32 flg = LB + L_FLG;
    const i32x4 rs_ws = mk_rsrc(ws, WS_USED);
    const __amdgpu_buffer_rsrc_t bws = __builtin_amdgcn_make_buffer_rsrc(ws, 0, WS_USED, 0x00020000);
    const u32 l16 = opaque(lane * 16);
    u32 spins = 0, pt = 0;
    u32 tag;
    f32x16 acc, zero;
#pragma unroll
    for (int i = 0; i < 16; i++) zero[i] = 0.f;
    const int erow = (hq & 1) * 32 + (tid >> 4);
    const int orow = rb * 64 + erow, ocol = (hq >> 1) * 128 + (tid & 15) * 8, og = rb * 2 + (hq & 1);
    u32x4 xe;
    u32 gp[4], xnp[4];
    auto calc_g = [&]() {
        const float e_m = lds_rf(LB + L_ST + 256 + erow * 4), e_rs = lds_rf(LB + L_ST + erow * 4);
#pragma unroll
        for (int e = 0; e < 4; e++) { xnp[e] = opaque(norm2(xe[e], e_m, e_rs)); gp[e] = opaque(pk_bf16(silu_(bf_lo(xnp[e])), silu_(bf_hi(xnp[e])))); }
    };
    auto ld_xe = [&]() { xe = *(const GAS u32x4*)(x + (size_t)orow * 512 + ocol); };
    auto st_flag = [&](u32 off) { __builtin_amdgcn_raw_buffer_store_b32(tag, bws, 0, off, 16); };
    auto get_tag = [&]() { tag = __builtin_amdgcn_readfirstlane(*(volatile LAS u32*)(flg + F_TAG * 4)); };
    constexpr u32 TP = 528;
    auto ffn_h = [&]() {
        if (b < 224) {
            const GAS u32* fg = (const GAS u32*)(ws + WS_FGU + ((b & ~7) + (lane & 7)) * FS);
            while (!__all(__hip_atomic_load(fg, RLX_AGENT) == tag) && ++spins <= SPIN_CAP) __builtin_amdgcn_s_sleep(4);
            __builtin_amdgcn_fence(__ATOMIC_ACQUIRE, "wavefront");
            STAMP(11);
            u32x4 gu[2][8];
#pragma unroll
            for (int j = 0; j < 2; j++)
#pragma unroll
                for (int i = 0; i < 8; i++) gu[j][i] = __builtin_amdgcn_raw_buffer_load_b128(bws, l16 + (wv - 2) * 2048 + j * 1024, WS_GU + (u32)((b & ~7) + i) * 4096, 16);
#pragma unroll
            for (int j = 0; j < 2; j++) {
                float a[4];
#pragma unroll
                for (int e = 0; e < 4; e++) { a[e] = __uint_as_float(gu[j][0][e]);
#pragma unroll
                    for (int i = 1; i < 8; i++) a[e] = np(a[e] + __uint_as_float(gu[j][i][e])); }
                const u32 gq = pk_bf16(a[0], a[2]), uq = pk_bf16(a[1], a[3]), sq = pk_bf16(silu_(bf_lo(gq)), silu_(bf_hi(gq)));
                const u32 hp = pk_bf16(bf_lo(sq) * bf_lo(uq), bf_hi(sq) * bf_hi(uq));
                const u32 ha = LB + L_H + (lane & 3) * 256 + (((wv - 2) * 2 + j) * 16 + (lane >> 2)) * 2;
                *(LAS u16*)ha = (u16)hp; *(LAS u16*)(ha + 128) = (u16)(hp >> 16);
            }
            lds_sig(flg + F_H * 4);
        }
    };
    auto gather_o = [&]() {
        lds_cnt(flg + F_WOF * 4, 4);
        calc_g();
        const u32 x2 = (rb & 7) * 2;
        const GAS u32* fq = (const GAS u32*)(ws + WS_FLAGO + (og * 16 + (((wv * 8 + (lane & 3) * 2) ^ x2) >> 1)) * FS);
        while (!__all(__hip_atomic_load(fq, RLX_AGENT) == tag) && ++spins <= SPIN_CAP) {}
        STAMP(26);
#pragma unroll
        for (int i = 0; i < 8; i++) dma16c(LB + L_OST + (wv * 8 + i) * 1024, l16, rs_ws, WS_O + (u32)(og * 32 + ((wv * 8 + i) ^ x2)) * 1024);
        WAITV(0); lds_sig(flg + F_OST * 4);
        STAMP(27);
        lds_cnt(flg + F_SEL * 4, 1);
        const u32 selk = *(LAS u32*)(LB + L_SEL + (lane & 7) * 4);
        for (u32 mine = (u32)__ballot(((2047u - (selk & 2047u)) >> 5) == (u32)og) & 0xffu; mine; mine &= mine - 1) {
            const int tk = __builtin_ctz(mine);
            const u32 yo = (u32)((tk * 8 + (hq >> 1) * 2 + h) * 28 + wv * 7) * 512 + l31 * 16;
            float y0, y1;
            for (;;) {
                u32x4 t[7];
#pragma unroll
                for (int i = 0; i < 7; i++) t[i] = __builtin_amdgcn_raw_buffer_load_b128(bws, yo + i * 512, WS_Y, 16);
                u32 d = 0; y0 = 0; y1 = 0;
#pragma unroll
                for (int i = 0; i < 7; i++) { d |= (t[i].y ^ tag) | (t[i].w ^ tag); y0 += __uint_as_float(t[i].x); y1 += __uint_as_float(t[i].z); }
                asm volatile("" ::: "memory");
                if (__all(d == 0u) || ++spins > SPIN_CAP) break;
            }
            *(LAS f32x2*)(LB + L_YR + tk * 2048 + wv * 512 + lane * 8) = (f32x2){y0, y1};
        }
        STAMP(24);
    };

    if (__builtin_expect(wv < 2, 1)) {
            __builtin_amdgcn_s_setprio(3);
            const int lw = wv;
            const u32 GB = LB + lw * GR;
            const i32x4 rx = mk_rsrc(x, 2048 * 512 * 2), rw = mk_rsrc(wqkv, 512 * 1536 * 2);
            const u32 vx0 = (u32)(lane >> 3) * 1024 + (((lane & 7) ^ ((lane >> 4) & 3)) << 4), vx1 = vx0 ^ 64;
            auto fill_x = [&](int bk) {
                u32 so = (u32)(rb * 64) * 1024 + (u32)((bk + rot) & 3) * 256 + lw * 128, ld = GB + bk * SL;
#pragma unroll
                for (int j = 0; j < 8; j++) { dma16(ld, (j & 1) ? vx1 : vx0, rx, so); ld += 1024; so += 8 * 1024; }
            };
            const u32 vw = (u32)(lane >> 3) * 3072 + (((lane & 7) ^ (((lane >> 4) & 1) << 2)) << 4);
            auto fill = [&](int f) {
                const int seg = (f >> 2) == 0 ? 1 : (f >> 2) == 1 ? 2 : 0;
                u32 so = ((u32)((f + rot) & 3) * 128 + lw * 64) * 3072 + seg * 1024 + hq * 128;
                u32 ld = GB + fill_slot(f) * SL;
#pragma unroll
                for (int j = 0; j < 8; j++) { dma16(ld, vw, rw, so); ld += 1024; so += 8 * 3072; }
            };
            fill_x(1); fill(1);
            WAITV(16); STAMP(1); RBAR();
            fill_x(2); fill(2);
            WAITV(16); RBAR();
            fill_x(3); fill(3);
            WAITV(16); RBAR();
            fill(4); fill(5);
            WAITV(16); STAMP(2); RBAR();
            fill(6); fill(7);
            WAITV(24); RBAR();
            WAITV(16); RBAR();
            fill(8);
            WAITV(8); STAMP(3); lds_sig(flg + F_LAND * 4);
            fill(9); fill(10);
            WAITV(8); lds_sig(flg + (F_LAND + 1) * 4);
            fill(11);
            WAITV(0); STAMP(4); lds_sig(flg + (F_LAND + 2) * 4);
            auto fill_o = [&](int sl) {
                const i32x4 ro = mk_rsrc(wo, 512 * 512 * 2);
                const u32 vo_w = (u32)(lane >> 4) * 1024 + (((lane & 15) ^ (((lane >> 4) & 3) << 2)) << 4);
                u32 so = (u32)(sl ^ (rb & 7)) * 32768 + (hq >> 1) * 256, ld = LB + sl * SL;
#pragma unroll
                for (int j = 0; j < 8; j++) { dma16(ld, vo_w, ro, so); ld += 1024; so += 4096; }
            };
            get_tag(); ld_xe();
            lds_cnt(flg + F_CSV * 4, 4);
            if (lw) {
#pragma unroll 1
                for (int i = 0; i < 2 * WO1; i++) { fill_o((i & 1) * 8 + (i & 2) * 2 + (i >> 2)); WAITV(WOQ); }
            } else {
                const int gg = rb * 2 - 8 + (lane & 7);
                const GAS u32* fp = (const GAS u32*)(ws + ((lane & 16) ? WS_FLAGV : WS_FLAGK) + ((gg < 0 ? 0 : gg) * 16 + hq * 2 + ((lane >> 3) & 1)) * FS);
                for (;;) {
                    const u32 ok = (u32)__ballot(gg < 0 || __hip_atomic_load(fp, RLX_AGENT) == tag);
                    const bool done = ok == 0xffffffffu || ++spins > SPIN_CAP;
                    if (lane == 0) *(volatile LAS u32*)(flg + F_HALO * 4) = done ? 0xffffffffu : ok;
                    if (done) break;
                }
                STAMP(7);
            }
            lds_cnt(flg + F_VREQ * 4, 4);
            STAMP(5);
#pragma unroll 1
            for (int i = WO1; i < 8; i++) { if (i == 7) lds_cnt(flg + F_FW * 4, 1); fill_o(lw * 8 + (i & 1) * 4 + (i >> 1)); WAITV(8); }
            WAITV(0); lds_sig(flg + F_WO * 4);
            STAMP(6);
            gather_o();
    } else if (__builtin_expect(wv >= 4, 1)) {
    __builtin_amdgcn_s_setprio(2);
    const int cw = wv - 4, rg = cw & 1, pr = cw >> 1;
    const int g = rb * 2 + rg, pair = hq * 2 + pr, g0 = rb * 2 - 8 + rg * 4;
    u32 xfa[2][4], wfa[2][4];
    {
        const int row = rg * 32 + l31;
        const u32 w0 = LB + (u32)(8 * h + ((lane & 15) >> 2)) * 128 + ((pr ^ ((lane >> 3) & 1)) << 6) + (((lane >> 4) & 1) << 5) + ((lane & 3) << 3);
#pragma unroll
        for (int s = 0; s < 4; s++) {
            xfa[0][s] = opaque(LB + (row >> 3) * 1024 + (row & 7) * 128 + (((2 * (s ^ cw) + h) ^ ((row >> 1) & 7)) << 4)); xfa[1][s] = opaque(xfa[0][s] + GR);
            wfa[0][s] = opaque(w0 + (s ^ cw) * 2048); wfa[1][s] = opaque(w0 + (s ^ cw) * 2048 + GR);
        }
    }
    const u32 csw = opaque(LB + L_CS + (rg * 64 + pr * 32 + ((lane & 1) << 4) + (lane >> 4) * 4) * 4), csr = opaque(LB + L_CS + (h * 64 + pr * 32 + l31) * 4);
    u32 hso[4];
#pragma unroll
    for (int t = 0; t < 4; t++) { hso[t] = g0 + t >= 0 ? (u32)((g0 + t) * 32 + pair * 2) * 1024 : 0x40000000u; asm volatile("" : "+s"(hso[t])); }
    u32 dmask[8];
    {
        const u32 mbits = (u32)((2ull << l31) - 1ull) >> (4 * h);
#pragma unroll
        for (int i = 0; i < 8; i++) { const int k = ((2 * i) & 3) + 8 * (i >> 1);
            dmask[i] = opaque(((u32)__builtin_amdgcn_sbfe(mbits, k, 1) & 0xffffu) | ((u32)__builtin_amdgcn_sbfe(mbits, k + 1, 1) & 0xffff0000u)); }
    }
    u32 own = rg ? LB + L_EXQ + cw * EXS : LB + L_OWN + pr * 4096;
    asm volatile("" : "+s"(own));
    const u32 vlo = h * 256 + (lane & 15) * 16;
    const u32 vab = opaque((lane & 16) ? WS_ONES + b * 2048 : WS_V + vlo), vam = opaque((lane & 16) ? 0x40000000u : 0xffffffffu);
    const u32 ownv = opaque((lane & 16) ? LB + L_ONES : own + 2048 + vlo);
    const u32 kbits = 0xfu << (pr * 8 + rg * 4), vbits = kbits << 16;
    const u32 selv = opaque((((lane >> 4) ^ lane) & 1) ? 0u : ONES);
    const u32x4 sel = {selv, selv, selv, selv};
    u32 blk = (u32)(g * 32 + pair * 2) * 1024, fo = (u32)(g * 16 + pair) * FS;
    asm volatile("" : "+s"(blk), "+s"(fo));
    const u32 vst = opaque(((lane >> 4) & 1) * 1024 + vlo);
    const u32 sta = opaque(LB + L_STP + (rg * 32 + l31) * 16);
    u32x4 xf[8][4], wf[2][4];
    f32x16 acc2;
    f32x4 cs;
    float sx0 = 0, sx1 = 0, sq0 = 0, sq1 = 0;
    auto rw_ = [&](int n, int s) {
        const u32 a = wfa[n & 1][s] + fill_slot(n >> 1) * SL;
        const u32x2 lo = tr_read(a), hi = tr_read(a + 512); wf[n & 1][s] = (u32x4){lo.x, lo.y, hi.x, hi.y};
        if (n < 8) xf[n][s] = lds_r128(xfa[n & 1][s] + (n >> 1) * SL);
    };
    auto mm_ = [&](int n, int s, f32x16& a) {
        const f32x16 c = ((n & 7) == 0 && s == 0) ? zero : a;
        a = (n >> 3) != 1 ? mfma32(wf[n & 1][s], xf[n & 7][s], c) : mfma32(xf[n & 7][s], wf[n & 1][s], c);
        PIN4(a);
    };
    auto cs_ = [&](int n, int s) { cs = mfma16(wf[n & 1][s], sel, ((n & 7) == 0 && s == 0) ? bc4(0.f) : cs); PIN4(cs); };
    auto st_ = [&](int i) {
#pragma unroll
        for (int s = 0; s < 2; s++) { const u32x4 v = xf[i][s];
            sx0 = dot2(v.x, ONES, sx0); sq0 = dot2(v.x, v.x, sq0); sx1 = dot2(v.y, ONES, sx1); sq1 = dot2(v.y, v.y, sq1);
            sx0 = dot2(v.z, ONES, sx0); sq0 = dot2(v.z, v.z, sq0); sx1 = dot2(v.w, ONES, sx1); sq1 = dot2(v.w, v.w, sq1); }
        asm volatile("" : "+v"(sx0), "+v"(sx1), "+v"(sq0), "+v"(sq1));
    };
#define RW(n, s) do { if ((n) < 24) rw_(n, s); } while (0)
#define STEP(n, A, X0, X1, X2, X3) do { STAMPS_STEP(n); \
        mm_(n, 0, A); SB(); RW(n + 1, 0); X0; SB(); cs_(n, 0); SB(); \
        mm_(n, 1, A); SB(); RW(n + 1, 1); X1; SB(); \
        mm_(n, 2, A); SB(); RW(n + 1, 2); X2; SB(); cs_(n, 2); SB(); \
        mm_(n, 3, A); SB(); RW(n + 1, 3); X3; SB(); if ((n) < 8) st_(n); if (!((n) & 1) && (n) <= 8) { if ((n) == 8) asm volatile("s_waitcnt lgkmcnt(8)" ::: "memory"); RBAR(); } SB(); } while (0)
#define NOP_ do {} while (0)
#define STEP0(n, A) STEP(n, A, NOP_, NOP_, NOP_, NOP_)
    u32 pk_;
#define PEEK(f) (pk_ = *(volatile LAS u32*)(flg + (f) * 4))
#define CHECK(f, n) ({ u32 hv_ = __builtin_amdgcn_readfirstlane(pk_); lds_need(hv_, flg + (f) * 4, (n) * (flg + (f) * 4)); })
    STAMP(1);
    RBAR();
    STAMP(2);
#pragma unroll
    for (int s = 0; s < 4; s++) rw_(0, s);
    SB();
    STEP0(0, acc); STEP0(1, acc); STEP0(2, acc); STEP0(3, acc); STEP0(4, acc); STEP0(5, acc); STEP0(6, acc); STEP0(7, acc);
    STAMP(5);
    {
        auto sw = __builtin_amdgcn_permlane32_swap(__float_as_uint(sx0 + sx1), __float_as_uint(sq0 + sq1), false, false);
        lds_wf(sta + h * 8 + pr * 4, __uint_as_float(sw[0]) + __uint_as_float(sw[1]));
        lds_wf4(csw, cs);
    }
    SB();
    float rs_t;
    u32x4 mfr;
    u32x4 kA, kB;
    {
        f32x4 sp; float mean, var, sd, c; u32 mp; u32x4 cf; u32x2 k0, k1;
        STEP0(8, acc2);
        sp = lds_rf4(sta); c = lds_rf(csr); SB();
        STEP(9, acc2, (mean = np(np(sp.x + sp.y) * (-1.0f / 512.0f)), mp = pk_bf16(mean, mean), mfr = (u32x4){mp, mp & 0xffffu, 0u, 0u}), (cf = split3(c)),
             (acc = mfma32(cf, mfr, acc), var = np(np(sp.z + sp.w) * (1.0f / 512.0f)), var = rbf(fmaxf(var - mean * mean, 0.f))),
             (sd = rbf(__builtin_amdgcn_sqrtf(rbf(var + 1e-5f))), rs_t = __builtin_amdgcn_rcpf(sd)));
        STEP(10, acc2, (PEEK(F_LAND), k0 = scl4(Q4(acc, 0), bc4(rs_t))), (k1 = scl4(Q4(acc, 1), bc4(rs_t)), kA = cat(k0, k1), st_sc1(bws, WS_K + blk + l16, kA)), (k0 = scl4(Q4(acc, 2), bc4(rs_t))),
             (k1 = scl4(Q4(acc, 3), bc4(rs_t)), kB = cat(k0, k1), st_sc1(bws, WS_K + 1024 + blk + l16, kB), CHECK(F_LAND, 2)));
        STAMP(6);
        STEP(11, acc2, (lds_wf(LB + L_ST + (rg * 32 + l31) * 4 + h * 256, h ? -bf_lo(mp) : rs_t)), NOP_, NOP_, NOP_);
    }
    u32 tg_;
    STEP(12, acc2, (tg_ = *(volatile LAS u32*)(flg + F_TAG * 4)), NOP_, NOP_, (tag = __builtin_amdgcn_readfirstlane(tg_))); STEP0(13, acc2);
    STEP(14, acc2, PEEK(F_LAND + 1), NOP_, NOP_, (WAITV(0), st_flag(WS_FLAGK + fo), CHECK(F_LAND + 1, 2)));
    STAMP(7);
    STEP0(15, acc2);
    lds_wf4(csw + 512, cs); lds_sig(flg + F_CSV * 4);
    u32x4 vS0, vS1;
    SB();
    {
        const u32 sa = LB + L_ST + (rg * 32 + 4 * h) * 4;
        f32x4 r0, r1, r2, r3; float c; u32x4 cf; u32x2 v0, v1;
        STEP(16, acc, NOP_, NOP_, PEEK(F_CSV), NOP_);
        CHECK(F_CSV, 4); c = lds_rf(csr + 512); SB();
        STEP(17, acc, (cf = split3(c)), (acc2 = mfma32(mfr, cf, acc2), r0 = lds_rf4(sa), r1 = lds_rf4(sa + 32)), (r2 = lds_rf4(sa + 64), r3 = lds_rf4(sa + 96)), (v0 = scl4(Q4(acc2, 0), r0)));
        STEP(18, acc, (PEEK(F_LAND + 2), v1 = scl4(Q4(acc2, 1), r1)), (vS0 = cat(v0, v1), st_sc1(bws, WS_V + blk + vst, vS0)), (v0 = scl4(Q4(acc2, 2), r2)),
             (v1 = scl4(Q4(acc2, 3), r3), vS1 = cat(v0, v1), st_sc1(bws, WS_V + 512 + blk + vst, vS1), CHECK(F_LAND + 2, 2)));
    }
    STAMP(8);
    STEP0(19, acc); STEP0(20, acc); STEP(21, acc, NOP_, PEEK(F_MW), NOP_, (WAITV(0), st_flag(WS_FLAGV + fo), CHECK(F_MW, 1)));
    STEP(22, acc, lds_w128(own + 2048 + vst, vS0), lds_w128(own + 2048 + 512 + vst, vS1), lds_w128(own + l16, kA), lds_w128(own + 1024 + l16, kB));
    STAMP(9);
    u32x4 kf[4][2], vf[4][2][2], vO[2][2];
    auto ld_ownv = [&](u32x4 (&v)[2][2]) {
#pragma unroll
        for (int j = 0; j < 4; j++) v[j >> 1][j & 1] = lds_r128(ownv + j * 512); };
    u32x4 qA, qB;
    float mA;
    {
        cs_(23, 0); cs_(23, 2); SB();
        mm_(23, 0, acc); SB(); lds_wf4(csw + 1024, cs); lds_sig(flg + F_CS * 4); SB();
        mm_(23, 1, acc); SB(); lds_bits(flg + F_HALO * 4, kbits);
#pragma unroll
        for (int hd = 0; hd < 2; hd++)
#pragma unroll
            for (int t = 0; t < 4; t++) kf[t][hd] = __builtin_amdgcn_raw_buffer_load_b128(bws, l16 + hd * 1024, WS_K + hso[t], 16);
        SB();
        STAMP(10);
        mm_(23, 2, acc); SB(); lds_cnt(flg + F_CS * 4, 4); const float c = lds_rf(csr + 1024); ld_ownv(vO); SB();
        mm_(23, 3, acc); SB();
        acc = mfma32(split3(c), mfr, acc);
        const f32x4 sF = bc4(rs_t * (0.25f * 1.4426950408889634f));
        qA = cat(scl4(Q4(acc, 0), sF), scl4(Q4(acc, 1), sF)); qB = cat(scl4(Q4(acc, 2), sF), scl4(Q4(acc, 3), sF));
        float m = dot2(qA.x, kA.x, 0.f), m2 = dot2(qB.x, kB.x, 0.f);
        m = dot2(qA.y, kA.y, m); m2 = dot2(qB.y, kB.y, m2); m = dot2(qA.z, kA.z, m); m2 = dot2(qB.z, kB.z, m2); m = dot2(qA.w, kA.w, m); m2 = dot2(qB.w, kB.w, m2);
        mA = -fmaxf(swap_add(m), swap_add(m2));
        const u32 e = LB + L_EXQ + cw * EXS;
        lds_w128(e + l16, qA); lds_w128(e + 1024 + l16, qB); lds_wf(e + 2048 + lane * 4, mA);
        lds_sig(flg + (F_Q + cw) * 4);
    }
    STAMP(11);
    f32x16 cm;
    auto set_cm = [&](float m) { asm volatile("" : "+v"(m));
#pragma unroll
        for (int i = 0; i < 16; i++) cm[i] = m; };
    auto half = [&](const f32x16& s, int hf, bool masked) {
        u32x4 p;
#pragma unroll
        for (int i = 0; i < 4; i++) { p[i] = pk_bf16(__builtin_amdgcn_exp2f(s[8 * hf + 2 * i]), __builtin_amdgcn_exp2f(s[8 * hf + 2 * i + 1])); if (masked) p[i] &= dmask[4 * hf + i]; }
        PIN4(p); return p; };
    f32x16 oA, oB, oC, oD, sa, sb;
    u32x4 p0, p1, pp[4][2];
#define TILE(S, MASK, P0, P1, M0, M1) do { P0 = half(S, 0, MASK); SB(); M0; SB(); P1 = half(S, 1, MASK); SB(); M1; SB(); } while (0)
    set_cm(mA); sb = mfp(kB, qB, cm); SB();
    sa = mfp(kA, qA, cm); SB();
    TILE(sb, true, p0, p1, (oB = mfp(vO[1][0], p0, zero)), (oB = mfp(vO[1][1], p1, oB)));
    STAMP(12);
    sb = mfp(kf[1][0], qA, cm); SB();
    STAMP(13);
    TILE(sa, true, p0, p1, (oA = mfp(vO[0][0], p0, zero)), (oA = mfp(vO[0][1], p1, oA)));
    {
        const u32 om = rg ? 0xffffffffu : 0u;
#pragma unroll
        for (int i = 0; i < 8; i++) dmask[i] = opaque(~dmask[i] | om);
    }
    SB();
    TILE(sb, false, pp[1][0], pp[1][1], (sa = mfp(kf[0][0], qA, cm)), (sb = mfp(kf[2][0], qA, cm)));
    TILE(sa, true, pp[0][0], pp[0][1], NOP_, (sa = mfp(kf[3][0], qA, cm)));
    asm volatile("" : "+v"(kf[0][1]), "+v"(kf[1][1]), "+v"(kf[2][1]), "+v"(kf[3][1]));
    STAMP(14);
    u32 vva[4];
#pragma unroll
    for (int t = 0; t < 4; t++) vva[t] = opaque(vab + (hso[t] & vam));
    lds_bits(flg + F_HALO * 4, vbits);
#pragma unroll
    for (int hd = 0; hd < 2; hd++)
#pragma unroll
        for (int t = 0; t < 4 - hd; t++) { vf[t][hd][0] = __builtin_amdgcn_raw_buffer_load_b128(bws, vva[t] + hd * 1024, 0, 16); vf[t][hd][1] = __builtin_amdgcn_raw_buffer_load_b128(bws, vva[t] + hd * 1024 + 512, 0, 16); }
    lds_sig(flg + F_VREQ * 4);
    SB();
    STAMP(15);
    TILE(sb, false, pp[2][0], pp[2][1], NOP_, (sb = mfp(kf[0][1], qB, cm)));
    TILE(sa, false, pp[3][0], pp[3][1], NOP_, (sa = mfp(kf[1][1], qB, cm)));
    TILE(sb, true, p0, p1, NOP_, (sb = mfp(kf[2][1], qB, cm)));
    STAMP(16);
    u32x4 p2, p3;
#define PVA(t, j) (oA = mfp(vf[t][0][j], pp[t][j], oA))
    PVA(0, 0); SB();
    STAMP(17);
    {
        auto quart = [&](const f32x16& s, int q, u32x4& p) { p[(2 * q) & 3] = pkp(__builtin_amdgcn_exp2f(s[4 * q]), __builtin_amdgcn_exp2f(s[4 * q + 1])); p[(2 * q + 1) & 3] = pkp(__builtin_amdgcn_exp2f(s[4 * q + 2]), __builtin_amdgcn_exp2f(s[4 * q + 3])); };
        quart(sa, 0, p2); SB(); PVA(0, 1); SB(); quart(sa, 1, p2); SB(); oB = mfp(vf[0][1][0], p0, oB); SB(); quart(sa, 2, p3); SB(); oB = mfp(vf[0][1][1], p1, oB); SB(); quart(sa, 3, p3); SB(); sa = mfp(kf[3][1], qB, cm); SB();
        quart(sb, 0, p0); SB(); PVA(1, 0); SB(); quart(sb, 1, p0); SB(); PVA(1, 1); SB();
        vf[3][1][0] = __builtin_amdgcn_raw_buffer_load_b128(bws, vva[3] + 1024, 0, 16); vf[3][1][1] = __builtin_amdgcn_raw_buffer_load_b128(bws, vva[3] + 1536, 0, 16); SB(); quart(sb, 2, p1); SB(); oB = mfp(vf[1][1][0], p2, oB); SB(); quart(sb, 3, p1); SB(); oB = mfp(vf[1][1][1], p3, oB); SB();
        asm volatile("" : "+v"(sa), "+v"(oA), "+v"(oB), "+v"(p0), "+v"(p1));
    }
    lds_cnt(flg + (F_Q + (cw ^ 1)) * 4, 1);
    const u32 ep = LB + L_EXQ + (cw ^ 1) * EXS;
    const u32x4 qC = lds_r128(ep + l16), qD = lds_r128(ep + 1024 + l16); const float mC = lds_rf(ep + 2048 + lane * 4);
    if (rg == 0) { kf[0][0] = lds_r128(own + l16); kf[0][1] = lds_r128(own + 1024 + l16); ld_ownv(vf[0]); }
    SB();
    {
        auto quart = [&](const f32x16& s, int q, u32x4& p) { p[(2 * q) & 3] = pkp(__builtin_amdgcn_exp2f(s[4 * q]), __builtin_amdgcn_exp2f(s[4 * q + 1])); p[(2 * q + 1) & 3] = pkp(__builtin_amdgcn_exp2f(s[4 * q + 2]), __builtin_amdgcn_exp2f(s[4 * q + 3])); };
        oB = mfp(vf[2][1][0], p0, oB); SB(); set_cm(mC); SB(); sb = mfp(kf[1][0], qC, cm); SB();
        quart(sa, 0, p2); SB(); oB = mfp(vf[2][1][1], p1, oB); SB(); quart(sa, 1, p2); SB(); PVA(2, 0); SB(); quart(sa, 2, p3); SB(); PVA(2, 1); SB(); quart(sa, 3, p3); SB(); sa = mfp(kf[2][0], qC, cm); SB();
        STAMP(18);
        p0 = half(sb, 0, true); SB(); oB = mfp(vf[3][1][0], p2, oB); SB(); PVA(3, 0); SB(); p1 = half(sb, 1, true); SB(); sb = mfp(kf[3][0], qC, cm); SB(); oB = mfp(vf[3][1][1], p3, oB); SB();
        quart(sa, 0, p2); SB(); PVA(3, 1); SB(); quart(sa, 1, p2); SB(); oC = mfp(vf[1][0][0], p0, zero); SB(); quart(sa, 2, p3); SB(); oC = mfp(vf[1][0][1], p1, oC); SB(); quart(sa, 3, p3); SB(); sa = mfp(kf[0][0], qC, cm); SB();
#define TILE3(S, MASK, PA, PB, M0, M1, M2) do { PA[0] = pkp(__builtin_amdgcn_exp2f(S[0]), __builtin_amdgcn_exp2f(S[1])); PA[1] = pkp(__builtin_amdgcn_exp2f(S[2]), __builtin_amdgcn_exp2f(S[3])); SB(); M0; SB(); \
            PA[2] = pkp(__builtin_amdgcn_exp2f(S[4]), __builtin_amdgcn_exp2f(S[5])); PA[3] = pkp(__builtin_amdgcn_exp2f(S[6]), __builtin_amdgcn_exp2f(S[7])); PB[0] = pkp(__builtin_amdgcn_exp2f(S[8]), __builtin_amdgcn_exp2f(S[9])); SB(); M1; SB(); \
            PB[1] = pkp(__builtin_amdgcn_exp2f(S[10]), __builtin_amdgcn_exp2f(S[11])); PB[2] = pkp(__builtin_amdgcn_exp2f(S[12]), __builtin_amdgcn_exp2f(S[13])); PB[3] = pkp(__builtin_amdgcn_exp2f(S[14]), __builtin_amdgcn_exp2f(S[15])); \
            if (MASK) { _Pragma("unroll") for (int i = 0; i < 4; i++) { PA[i] &= dmask[i]; PB[i] &= dmask[4 + i]; } } SB(); M2; SB(); } while (0)
        TILE3(sb, false, p0, p1, (oC = mfp(vf[2][0][0], p2, oC)), (oC = mfp(vf[2][0][1], p3, oC)), (sb = mfp(kf[1][1], qD, cm)));
        TILE3(sa, false, p2, p3, (oC = mfp(vf[3][0][0], p0, oC)), (oC = mfp(vf[3][0][1], p1, oC)), (sa = mfp(kf[2][1], qD, cm)));
        TILE3(sb, true, p0, p1, (oC = mfp(vf[0][0][0], p2, oC)), (oC = mfp(vf[0][0][1], p3, oC)), (sb = mfp(kf[3][1], qD, cm)));
        TILE3(sa, false, p2, p3, (oD = mfp(vf[1][1][0], p0, zero)), (oD = mfp(vf[1][1][1], p1, oD)), (sa = mfp(kf[0][1], qD, cm)));
        lds_wf4(ep + l16, Q4(oC, 0)); lds_wf4(ep + 1024 + l16, Q4(oC, 1)); SB();
        TILE3(sb, false, p0, p1, (oD = mfp(vf[2][1][0], p2, oD)), (oD = mfp(vf[2][1][1], p3, oD)), NOP_);
        p2 = half(sa, 0, false); SB(); oD = mfp(vf[3][1][0], p0, oD); SB(); oD = mfp(vf[3][1][1], p1, oD); SB();
        p3 = half(sa, 1, false); SB(); oD = mfp(vf[0][1][0], p2, oD); oD = mfp(vf[0][1][1], p3, oD);
    }
    lds_wf4(ep + 2048 + l16, Q4(oD, 0)); lds_wf4(ep + 3072 + l16, Q4(oD, 1)); *(LAS f32x2*)(ep + 4096 + lane * 8) = (f32x2){oC[8], oD[8]};
    lds_sig(flg + (F_O + cw) * 4);
    STAMP(19);
    ld_xe();
    lds_cnt(flg + (F_O + (cw ^ 1)) * 4, 1);
    STAMP(20);
    {
        const u32 e = LB + L_EXQ + cw * EXS;
        const f32x4 t0 = lds_rf4(e + l16), t1 = lds_rf4(e + 1024 + l16), t2 = lds_rf4(e + 2048 + l16), t3 = lds_rf4(e + 3072 + l16); const f32x2 tl = *(LAS f32x2*)(e + 4096 + lane * 8);
        float l[2] = {oA[8] + tl.x, oB[8] + tl.y};
        f32x4 o0[2] = {add4(Q4(oA, 0), t0), add4(Q4(oB, 0), t2)}, o1[2] = {add4(Q4(oA, 1), t1), add4(Q4(oB, 1), t3)};
        u32x4 op[2];
#pragma unroll
        for (int hd = 0; hd < 2; hd++) { const f32x4 inv = bc4(__builtin_amdgcn_rcpf(l[hd])); op[hd] = cat(scl4(o0[hd], inv), scl4(o1[hd], inv)); }
        u32 nf = 0;
#pragma unroll
        for (int i = 0; i < 8; i++) { const u32 v = op[i >> 2][i & 3]; nf |= ((v & 0x7f807f80u) + 0x00800080u); }
        if (__builtin_expect(FORCE_FB || !__all(l[0] >= 0x1p-60f && l[0] <= 0x1p60f && l[1] >= 0x1p-60f && l[1] <= 0x1p60f && (nf & 0x80008000u) == 0u), 0)) {
#pragma unroll 1
          for (int hd = 0; hd < 2; hd++) {
            const u32x4 qF = hd ? qB : qA;
            const int j0 = g >= 8 ? g - 8 : 0, head = pair * 2 + hd;
            { int gg = j0 + (lane & 15); gg = gg > g ? g : gg;
              const GAS u32* fp = (const GAS u32*)(ws + ((lane & 16) ? WS_FLAGV : WS_FLAGK) + (gg * 16 + pair) * FS);
              while (!__all(__hip_atomic_load(fp, RLX_AGENT) == tag) && ++spins <= SPIN_CAP) {}
              __builtin_amdgcn_fence(__ATOMIC_ACQUIRE, "wavefront"); }
            float mx = -3.0e38f;
            f32x16 o = zero;
#pragma unroll 1
            for (int pass = 0; pass < 2; pass++) {
#pragma unroll 1
                for (int j = j0; j <= g; j++) {
                    const u32 bo = (u32)(j * 32 + head) * 1024;
                    const f32x16 sc = mfma32(__builtin_amdgcn_raw_buffer_load_b128(bws, l16, WS_K + bo, 16), qF, zero);
                    const int dq = (g - j) * 32 + l31 - 4 * h;
                    float p[16];
#pragma unroll
                    for (int i = 0; i < 16; i++) { const int d = dq - (i & 3) - 8 * (i >> 2); const bool vis = d >= 0 && d < 256;
                        if (vis) mx = pass ? mx : fmaxf(mx, sc[i]); p[i] = vis ? __builtin_amdgcn_exp2f(sc[i] - mx) : 0.f; }
                    if (pass) {
                        const u32 va = (lane & 16) ? WS_ONES + b * 2048 : WS_V + vlo + bo;
                        o = mfma32(__builtin_amdgcn_raw_buffer_load_b128(bws, va, 0, 16), (u32x4){pk_bf16(p[0], p[1]), pk_bf16(p[2], p[3]), pk_bf16(p[4], p[5]), pk_bf16(p[6], p[7])}, o);
                        o = mfma32(__builtin_amdgcn_raw_buffer_load_b128(bws, va + 512, 0, 16), (u32x4){pk_bf16(p[8], p[9]), pk_bf16(p[10], p[11]), pk_bf16(p[12], p[13]), pk_bf16(p[14], p[15])}, o);
                    }
                }
                auto sw = __builtin_amdgcn_permlane32_swap(__float_as_uint(mx), __float_as_uint(mx), false, false);
                mx = fmaxf(__uint_as_float(sw[0]), __uint_as_float(sw[1]));
            }
            const f32x4 inv = bc4(__builtin_amdgcn_rcpf(o[8]));
            const u32x4 r = cat(scl4(Q4(o, 0), inv), scl4(Q4(o, 1), inv));
            if (hd) op[1] = r; else op[0] = r;
          }
        }
        st_sc1(bws, WS_O + blk + l16, op[0]); st_sc1(bws, WS_O + 1024 + blk + l16, op[1]);
    }
    STAMP(21);
    SB();
    lds_cnt(flg + F_WO * 4, 2);
    u32x4 wof[32];
    {
        const int q = (lane & 15) >> 2;
        const u32 a = LB + (u32)(4 * h + q) * 256 + ((cw ^ q) << 6) + (((lane >> 4) & 1) << 5) + ((lane & 3) << 3), a2 = opaque(a + GR);
#pragma unroll
        for (int s = 0; s < 32; s++) { const u32 c = (s < 16 ? a : a2) + (s & 15) * 4096; wof[s] = cat(tr_read(c), tr_read(c + 2048)); }
        lds_sig(flg + F_WOF * 4);
    }
    SB();
    WAITV(0); st_flag(WS_FLAGO + fo);
    STAMP(22);
    SB();
    calc_g();
    STAMP(25);
    {
        u32x4 of[8];
        const u32 oa = opaque(LB + L_OST + l16);
        lds_cnt(flg + F_OST * 4, 4);
        STAMP(28);
#pragma unroll
        for (int s = 0; s < 8; s++) of[s] = lds_r128(oa + s * 1024);
        SB();
#pragma unroll
        for (int s = 0; s < 32; s++) { acc = mfp(wof[s], of[s & 7], s ? acc : zero); SB(); if (s < 24) of[s & 7] = lds_r128(oa + (s + 8) * 1024); SB(); }
        const u32 a = LB + L_TR + l31 * TP + cw * 128 + h * 16;
#pragma unroll
        for (int q = 0; q < 4; q++) lds_wf4(a + q * 32, Q4(acc, q));
    }
    } else if (wv == 2) {
                __builtin_amdgcn_s_setprio(3);
                const u32x4 o4 = {ONES, ONES, ONES, ONES};
                *(LAS u32*)(LB + L_FLG + lane * 4) = 0u;
                const u32 ep_v = __hip_atomic_load((const GAS u32*)(ws + WS_EPOCH), RLX_AGENT);
                lds_w128(LB + L_ONES + l16, o4); lds_w128(LB + L_ONES + 1024 + l16, o4);
                st_sc1(bws, WS_ONES + b * 2048 + l16, o4); st_sc1(bws, WS_ONES + b * 2048 + 1024 + l16, o4);
                ld_xe();
                BAR();
                *(volatile LAS u32*)(flg + F_TAG * 4) = ep_v + 1u;
                tag = __builtin_amdgcn_readfirstlane(ep_v) + 1u;
                BAR();
                auto ld_fw = [&](int mt, u32 ld) {
                    const u32 sw = ((lane & 7) ^ (((lane >> 4) & 1) << 2)) << 4, rp = mt < 2 ? 3584 : 1024;
                    const i32x4 rf = mk_rsrc(mt == 0 ? wg : mt == 1 ? wu : wd, b < 224 ? 512 * 1792 * 2 : 0);
                    u32 so = mt < 2 ? (u32)(b & 7) * 64 * 3584 + (u32)(b >> 3) * 128 : (u32)(b >> 3) * 65536 + (u32)(b & 7) * 128;
#pragma unroll
                    for (int j = 0; j < 8; j++) { dma16(ld, (u32)(lane >> 3) * rp + sw, rf, so); ld += 1024; so += 8 * rp; } };
                u32x4 fw[3][4][2];
                auto rd_fw = [&](int mt, u32 base) {
                    const u32 wa = base + (u32)(8 * (lane >> 4) + ((lane & 15) >> 2)) * 128 + ((lane & 3) << 3), sw = ((lane >> 3) & 1) << 6;
#pragma unroll
                    for (int ct = 0; ct < 4; ct++) { const u32 a = wa + ((((ct >> 1) << 6) ^ sw) + (ct & 1) * 32);
                        fw[mt][ct][0] = cat(tr_read(a), tr_read(a + 512)); fw[mt][ct][1] = cat(tr_read(a + 4096), tr_read(a + 4096 + 512)); } };
                RBAR(); RBAR(); RBAR();
                ld_fw(0, LB + L_FW); ld_fw(1, LB + L_FW + SL);
                RBAR();
                WAITV(0);
                STAMP(4);
                rd_fw(0, LB + L_FW); rd_fw(1, LB + L_FW + SL);
                lds_sig(flg + F_MW * 4);
                if (b < 224) {
                    lds_cnt(flg + F_SEL * 4, 1);
                    const u32 sel = *(volatile LAS u32*)(LB + L_SEL + (lane & 7) * 4);
                    const int srow = 2047 - (int)(sel & 2047u);
                    const u16* xp = x + (size_t)srow * 512 + (b & 7) * 64 + (lane >> 4) * 8;
                    u32x2 st = __builtin_amdgcn_raw_buffer_load_b64(bws, WS_S + srow * 8, 0, 16);
                    u32x4 xs0 = *(const GAS u32x4*)xp, xs1 = *(const GAS u32x4*)(xp + 32);
                    asm volatile("" : "+v"(xs0), "+v"(xs1) :: "memory");
                    while (!__all(st.y == tag) && ++spins <= SPIN_CAP) { st = __builtin_amdgcn_raw_buffer_load_b64(bws, WS_S + srow * 8, 0, 16); asm volatile("" ::: "memory"); }
                    STAMP(10);
                    const float m = bf_lo(st.x), rs = __builtin_amdgcn_rcpf(bf_hi(st.x));
#pragma unroll
                    for (int e = 0; e < 4; e++) { xs0[e] = norm2(xs0[e], m, rs); xs1[e] = norm2(xs1[e], m, rs); }
                    const u32 go = WS_GU + (u32)b * 4096 + ((lane >> 4) * 32 + (lane & 8) * 2 + (lane & 7)) * 8;
                    const u32 hi8 = (lane & 8) ? 0xffffffffu : 0u;
#pragma unroll
                    for (int ct = 0; ct < 4; ct++) {
                        const f32x4 d0 = mfma16(fw[0][ct][1], xs1, mfma16(fw[0][ct][0], xs0, bc4(0.f))), d1 = mfma16(fw[1][ct][1], xs1, mfma16(fw[1][ct][0], xs0, bc4(0.f)));
#pragma unroll
                        for (int e = 0; e < 2; e++)
                            __builtin_amdgcn_raw_buffer_store_b64((u32x2){(__float_as_uint(d0[2 + e]) & hi8) | (__float_as_uint(d0[e]) & ~hi8), (__float_as_uint(d1[2 + e]) & hi8) | (__float_as_uint(d1[e]) & ~hi8)}, bws, go + (ct * 16 + e) * 64, 0, 16);
                    }
                    WAITV(0); st_flag(WS_FGU + b * FS);
                    STAMP(9);
                    lds_cnt(flg + F_VREQ * 4, 4);
                    ld_fw(2, LB + 15 * SL); WAITV(0); rd_fw(2, LB + 15 * SL); lds_sig(flg + F_FW * 4);
                    ffn_h();
                    lds_cnt(flg + F_H * 4, 2);
                    const u32x4 h0 = lds_r128(LB + L_H + (lane & 7) * 128 + (lane >> 4) * 16), h1 = lds_r128(LB + L_H + (lane & 7) * 128 + 64 + (lane >> 4) * 16);
                    const u32 yo = WS_Y + (u32)(((lane & 7) * 8 + (b & 7)) * 28 + (b >> 3)) * 512 + (lane >> 4) * 32 + (lane & 8) * 2;
#pragma unroll
                    for (int ct = 0; ct < 4; ct++) {
                        const f32x4 d = mfma16(fw[2][ct][1], h1, mfma16(fw[2][ct][0], h0, bc4(0.f)));
                        st_sc1(bws, yo + ct * 128, (u32x4){(__float_as_uint(d.z) & hi8) | (__float_as_uint(d.x) & ~hi8), tag, (__float_as_uint(d.w) & hi8) | (__float_as_uint(d.y) & ~hi8), tag});
                    }
                    STAMP(12);
                } else lds_sig(flg + F_FW * 4);
                gather_o();
    } else {
                __builtin_amdgcn_s_setprio(3);
                const int rrow = rb * 64 + hq * 8 + (lane >> 3);
                u32x4 xr[8], wr_[8];
#pragma unroll
                for (int i = 0; i < 8; i++) { xr[i] = *(const GAS u32x4*)(x + (size_t)rrow * 512 + i * 64 + (lane & 7) * 8); wr_[i] = *(const GAS u32x4*)(wr + i * 64 + (lane & 7) * 8); }
                BAR();
                float a0 = 0, a1 = 0, a2 = 0, a3 = 0;
#pragma unroll
                for (int i = 0; i < 8; i++) {
#pragma unroll
                    for (int e = 0; e < 4; e++) { a0 = dot2(xr[i][e], ONES, a0); a1 = dot2(xr[i][e], xr[i][e], a1); a2 = dot2(xr[i][e], wr_[i][e], a2); a3 = dot2(wr_[i][e], ONES, a3); }
                    if (i == 3) { asm volatile("" : "+v"(a0), "+v"(a1), "+v"(a2), "+v"(a3)); RBAR(); }
                }
                asm volatile("" : "+v"(a0), "+v"(a1), "+v"(a2), "+v"(a3));
                RBAR();
                tag = __builtin_amdgcn_readfirstlane(*(volatile LAS u32*)(flg + F_TAG * 4));
                {
                    const float mean = red8(a0) * (1.0f / 512.0f), var = fmaxf(red8(a1) * (1.0f / 512.0f) - mean * mean, 0.f);
                    const float m = rbf(mean), sd = rbf(__builtin_amdgcn_sqrtf(rbf(rbf(var) + 1e-5f)));
                    const u32 rbits = pk_bf16((red8(a2) - m * red8(a3)) * __builtin_amdgcn_rcpf(sd), 0.f) & 0xffffu;
                    const u32 key = (((rbits & 0x8000u) ? (~rbits & 0xffffu) : (rbits | 0x8000u)) << 11) | (u32)(2047 - rrow);
                    if ((lane & 7) < 2) __builtin_amdgcn_raw_buffer_store_b64((u32x2){(lane & 1) ? pk_bf16(m, sd) : key, tag}, bws, ((lane & 1) ? WS_S : WS_R) + rrow * 8, 0, 16);
                }
                RBAR(); RBAR(); RBAR();
                u32x4 kq[16];
                auto gather = [&]() {
#pragma unroll
                    for (int i = 0; i < 16; i++) kq[i] = __builtin_amdgcn_raw_buffer_load_b128(bws, l16 + i * 1024, WS_R, 16);
                    asm volatile("" ::: "memory"); };
                auto stale = [&]() { u32 d = 0;
#pragma unroll
                    for (int i = 0; i < 16; i++) d |= (kq[i].y ^ tag) | (kq[i].w ^ tag);
                    return !__all(d == 0u); };
                gather();
                while (stale() && ++spins <= SPIN_CAP) gather();
                STAMP(7);
                u32 m1 = 0, m2 = 0, m3 = 0;
#pragma unroll
                for (int i = 0; i < 32; i++) { const u32 k = kq[i >> 1][(i & 1) * 2];
                    asm("v_med3_u32 %0, %1, %0, %2" : "+v"(m3) : "v"(m2), "v"(k)); asm("v_med3_u32 %0, %1, %0, %2" : "+v"(m2) : "v"(m1), "v"(k)); m1 = m1 > k ? m1 : k; }
                u32 thr = 0;
#pragma unroll 1
                for (u32 bit = 1u << 26; bit; bit >>= 1) {
                    const u32 t = thr | bit;
                    const int c = __builtin_popcountll(__ballot(m1 >= t)) + __builtin_popcountll(__ballot(m2 >= t));
                    if (c >= 8) { thr = t; if (c == 8) break; }
                }
                if (__builtin_expect(FORCE_SEL || __any(m3 >= thr), 0)) {
                    thr = 0;
#pragma unroll 1
                    for (u32 bit = 1u << 26; bit; bit >>= 1) {
                        const u32 t = thr | bit; int c = 0;
#pragma unroll
                        for (int i = 0; i < 32; i++) c += __builtin_popcountll(__ballot(kq[i >> 1][(i & 1) * 2] >= t));
                        if (c >= 8) thr = t;
                    }
                    int pos = 0;
#pragma unroll
                    for (int i = 0; i < 32; i++) { const u32 k = kq[i >> 1][(i & 1) * 2]; const u64 bm = __ballot(k >= thr);
                        const int p = pos + __builtin_amdgcn_mbcnt_hi((u32)(bm >> 32), __builtin_amdgcn_mbcnt_lo((u32)bm, 0u));
                        if (k >= thr) *(LAS u32*)(LB + L_SEL + (p & 7) * 4) = k; pos += __builtin_popcountll(bm); }
                } else {
                    const u64 b1 = __ballot(m1 >= thr), b2 = __ballot(m2 >= thr);
                    const int p1 = __builtin_amdgcn_mbcnt_hi((u32)(b1 >> 32), __builtin_amdgcn_mbcnt_lo((u32)b1, 0u));
                    const int p2 = __builtin_popcountll(b1) + __builtin_amdgcn_mbcnt_hi((u32)(b2 >> 32), __builtin_amdgcn_mbcnt_lo((u32)b2, 0u));
                    if (m1 >= thr) *(LAS u32*)(LB + L_SEL + (p1 & 7) * 4) = m1;
                    if (m2 >= thr) *(LAS u32*)(LB + L_SEL + (p2 & 7) * 4) = m2;
                }
                asm volatile("" ::: "memory");
                lds_sig(flg + F_SEL * 4);
                STAMP(8);
                ld_xe();
                lds_cnt(flg + F_VREQ * 4, 4);
                ffn_h();
                if (b == 0) {
                    for (;;) { bool ok = true;
#pragma unroll
                        for (int i = 0; i < 4; i++) { const int w = lane + 64 * i; ok &= __hip_atomic_load((const GAS u32*)(ws + WS_FLAGK + ((w >> 3) * 2 * 16 + (w & 7) * 2) * FS), RLX_AGENT) == tag; }
                        if (__all(ok) || ++spins > SPIN_CAP) break;
                    }
                    st_flag(WS_EPOCH);
                }
                gather_o();
    }

    STAMP(29);
    SB(); BAR();
    STAMP(30);
    {
        const u32 a = LB + L_TR + (tid >> 4) * TP + (tid & 15) * 32;
        const u32x4 sp4 = pack8(lds_rf4(a), lds_rf4(a + 16));
        const u32 selk = *(LAS u32*)(LB + L_SEL + (lane & 7) * 4);
        for (u32 mm = (u32)__ballot(((2047u - (selk & 2047u)) >> 2) == (u32)(og * 8 + wv)) & 0xffu; mm; mm &= mm - 1) {
            const int tk = __builtin_ctz(mm);
            const u32 key = __builtin_amdgcn_readlane(selk, tk);
            if (2047 - (int)(key & 2047u) == orow) {
                const u32 k16 = key >> 11, rbits = (k16 & 0x8000u) ? (k16 & 0x7fffu) : (~k16 & 0xffffu);
                const float rsel = rbf(__builtin_amdgcn_rcpf(1.0f + __builtin_amdgcn_exp2f(bf_lo(rbits) * -1.4426950408889634f)));
                const u32 ya = LB + L_YR + tk * 2048 + (tid & 15) * 32;
                const u32x4 yp = pack8((lds_rf4(ya) + lds_rf4(ya + 512)) + (lds_rf4(ya + 1024) + lds_rf4(ya + 1536)), (lds_rf4(ya + 16) + lds_rf4(ya + 528)) + (lds_rf4(ya + 1040) + lds_rf4(ya + 1552)));
#pragma unroll
                for (int e = 0; e < 4; e++) {
                    const u32 yy = pk_bf16(bf_lo(yp[e]) * rsel, bf_hi(yp[e]) * rsel);
                    const u32 xb = pk_bf16(bf_lo(xnp[e]) + bf_lo(yy), bf_hi(xnp[e]) + bf_hi(yy));
                    gp[e] = pk_bf16(silu_(bf_lo(xb)), silu_(bf_hi(xb)));
                }
            }
        }
        u32x4 ov;
#pragma unroll
        for (int e = 0; e < 4; e++) {
            const u32 t = pk_bf16(bf_lo(sp4[e]) * bf_lo(gp[e]), bf_hi(sp4[e]) * bf_hi(gp[e]));
            ov[e] = pk_bf16(bf_lo(xe[e]) + bf_lo(t), bf_hi(xe[e]) + bf_hi(t));
        }
        __builtin_amdgcn_raw_buffer_store_b128(ov, __builtin_amdgcn_make_buffer_rsrc(out, 0, 2048 * 512 * 2, 0x00020000), (u32)(orow * 512 + ocol) * 2, 0, OUTAUX);
    }
}
#define STAMP_WAVES 8
#define STAMP_NAMES {"start","pre/L:x0 in","P0 ok/L:Wk in","L:Wv in","L:Wq in","K mfma/L:Vreq/P:halo","K stored/L:Wo in","flagK/R:gath","V stored/R:sel","flagV/R:A1","K issued/R:xs","Q done/R:GUok","diagB/R:A2","K landed","P01","V issued","P23","PV A","F done","S done","O ok","o stored","flagO","","P:y done","g done","L:o poll ok","L:o in/P:y","o q0 ok","arr Bs","rel Bs"}

extern "C" void kernel_launch(void* const* d_in, const int* in_sizes, int n_in, void* d_out, int out_size, void* d_ws, size_t ws_size, hipStream_t stream) {
    fused_kernel<<<256, 512, 0, stream>>>((const u16*)d_in[0], (const u16*)d_in[1], (char*)d_ws, (const u16*)d_in[2], (u16*)d_out, (const u16*)d_in[4], (const u16*)d_in[5], (const u16*)d_in[6], (const u16*)d_in[7]);
}
```
